```python
import torch

def _softmax(x, axis=-1):
    return torch.softmax(x, dim=axis)
def _gelu(x):
    return torch.nn.functional.gelu(x, approximate="tanh")
def _silu(x):
    return torch.nn.functional.silu(x)
def _sigmoid(x):
    return torch.sigmoid(x)
def _layernorm(x, axis=-1, eps=1e-5):
    m = x.mean(dim=axis, keepdim=True)
    v = x.var(dim=axis, keepdim=True, unbiased=False)
    return (x - m) / torch.sqrt(v + eps)
def _scatter_add(tgt, idx, src, axis=0):
    return tgt.clone().index_add_(axis, idx, src)
def _segment_sum(src, seg, n):
    out = torch.zeros((n,) + src.shape[1:], dtype=src.dtype)
    return out.index_add_(0, seg, src)
def _pad(x, pw):
    flat = tuple(v for pair in reversed(pw) for v in pair)
    return torch.nn.functional.pad(x, flat)

kernel_name = 'concat_heads__sliding_local_dilated__grouped_kv_mqa__prompt'

def setup_inputs():

    _w = (lambda a: (lambda t: t.to(torch.bfloat16) if t.is_floating_point() else t)(torch.as_tensor(a)))
    def _A_setup():
        B, S, D = 1, 2048, 576
        return {
            "x": _w((lambda *s: torch.randn(*s, dtype=torch.bfloat16) * 0.1)(B, S, D)),
            "wqkv": _w((lambda *s: torch.randn(*s, dtype=torch.bfloat16) * 0.8)(D, 3 * D)),
            "wo": _w((lambda *s: torch.randn(*s, dtype=torch.bfloat16) * 0.8)(D, D)),
        }
    _A_in = _A_setup()

    def _B_setup():
        B, S, D, H = 1, 2048, 576, 32
        Dh = D // H
        return {
            "x": _w((lambda *s: torch.randn(*s, dtype=torch.bfloat16) * 0.05)(B, S, D)),
            "wq": _w((lambda *s: torch.randn(*s, dtype=torch.bfloat16) * 3.2)(D, H * Dh)),
            "wk": _w((lambda *s: torch.randn(*s, dtype=torch.bfloat16) * 3.2)(D, Dh)),
            "wv": _w((lambda *s: torch.randn(*s, dtype=torch.bfloat16) * 1.6)(D, Dh)),
            "wo": _w((lambda *s: torch.randn(*s, dtype=torch.bfloat16) * 1.6)(H * Dh, D)),
        }
    _B_in = _B_setup()

    return {
        "A_x": _A_in["x"],
        "A_wqkv": _A_in["wqkv"],
        "A_wo": _A_in["wo"],
        "B_x": _B_in["x"],
        "B_wq": _B_in["wq"],
        "B_wk": _B_in["wk"],
        "B_wv": _B_in["wv"],
        "B_wo": _B_in["wo"],
    }

def reference(A_x, A_wqkv, A_wo, B_x, B_wq, B_wk, B_wv, B_wo):
    def _A_ref(x, wqkv, wo):
        B, S, D = x.shape
        H = 32; Dh = D // H
        W, dil = 8, 2
        qkv = (lambda x, *d: x.permute(*d))((x @ wqkv).reshape(B, S, 3, H, Dh), 2, 0, 3, 1, 4)
        q, k, v = qkv[0], qkv[1], qkv[2]
        scale = Dh ** -0.5
        pos = torch.arange(S)
        off = torch.arange(W) * dil
        idx = pos[:, None] - off[None, :]
        mask = idx >= 0
        idx_c = torch.where(mask, idx, 0).reshape(-1)
        kg = k[:, :, idx_c, :].reshape(B, H, S, W, Dh)
        vg = v[:, :, idx_c, :].reshape(B, H, S, W, Dh)
        logits = torch.einsum("bhsd,bhswd->bhsw", q, kg) * scale
        logits = torch.where(mask[None, None, :, :], logits, -1e9)
        attn = _softmax(logits, axis=-1)
        o = torch.einsum("bhsw,bhswd->bhsd", attn, vg)
        return (lambda x, *d: x.permute(*d))(o, 0, 2, 1, 3).reshape(B, S, D) @ wo

    def _B_ref(x, wq, wk, wv, wo):
        B, S, D = x.shape
        H = 32
        Dh = D // H
        q = (lambda x, *d: x.permute(*d))((x @ wq).reshape(B, S, H, Dh), 0, 2, 1, 3)
        k = x @ wk
        v = x @ wv
        scores = torch.einsum("bhqd,bkd->bhqk", q, k) * (Dh ** -0.5)
        p = _softmax(scores, axis=-1)
        o = torch.einsum("bhqk,bkd->bhqd", p, v)
        return (lambda x, *d: x.permute(*d))(o, 0, 2, 1, 3).reshape(B, S, H * Dh) @ wo

    a = _A_ref(A_x, A_wqkv, A_wo)
    b = _B_ref(B_x, B_wq, B_wk, B_wv, B_wo)
    return torch.concatenate([a, b], axis=-1)
```

```cpp
#include <hip/hip_runtime.h>
#include <hip/hip_bf16.h>
#include <stdint.h>

typedef __bf16 bf16x2 __attribute__((ext_vector_type(2)));
typedef __bf16 bf16x8 __attribute__((ext_vector_type(8)));
typedef short s16x4 __attribute__((ext_vector_type(4)));
typedef short s16x8 __attribute__((ext_vector_type(8)));
typedef float f32x2 __attribute__((ext_vector_type(2)));
typedef float f32x4 __attribute__((ext_vector_type(4)));
typedef float f32x16 __attribute__((ext_vector_type(16)));
typedef unsigned u32x2 __attribute__((ext_vector_type(2)));
typedef unsigned u32x4 __attribute__((ext_vector_type(4)));
#define LDSP(T) __attribute__((address_space(3))) T*
#define DI __device__ __forceinline__

constexpr int SEQ = 2048, DM = 576, NH = 32, DH = 18;
constexpr int ROWB = DM * 2;
constexpr float QSCALE = 0.23570226039551584f * 1.4426950408889634f;

constexpr size_t WS_QM = 0;
constexpr size_t WS_QR = WS_QM + (size_t)SEQ * 1024;
constexpr size_t WS_KV = WS_QR + (size_t)SEQ * 128;
constexpr int CHUNK_B = 19456, KREM_OFF = 8192, V_OFF = 9216, VTILE_B = 1280;
constexpr int KV_BYTES = 8 * CHUNK_B;
constexpr size_t WS_OA = WS_KV + KV_BYTES;
constexpr size_t WS_OB = WS_OA + (size_t)SEQ * ROWB;

DI unsigned pk2(float a, float b) {
  f32x2 f = {a, b};
  return __builtin_bit_cast(unsigned, __builtin_convertvector(f, bf16x2));
}
DI float dot2(unsigned a, unsigned b, float c) {
  return __builtin_amdgcn_fdot2_f32_bf16(__builtin_bit_cast(bf16x2, a), __builtin_bit_cast(bf16x2, b), c, false);
}
DI float bf_lo(unsigned u) { return __uint_as_float(u << 16); }
DI float bf_hi(unsigned u) { return __uint_as_float(u & 0xffff0000u); }

DI void glds16(const void* gsrc, unsigned lds_dst) {
  unsigned keep;
  asm volatile("s_mov_b32 %0, m0\n\ts_mov_b32 m0, %2\n\ts_nop 0\n\tglobal_load_lds_dwordx4 %1, off\n\ts_mov_b32 m0, %0"
               : "=&s"(keep) : "v"(gsrc), "s"(lds_dst) : "memory");
}
DI void glds4(const void* gsrc, unsigned lds_dst) {
  unsigned keep;
  asm volatile("s_mov_b32 %0, m0\n\ts_mov_b32 m0, %2\n\ts_nop 0\n\tglobal_load_lds_dword %1, off\n\ts_mov_b32 m0, %0"
               : "=&s"(keep) : "v"(gsrc), "s"(lds_dst) : "memory");
}
template <int N> DI void wait_vm() { asm volatile("s_waitcnt vmcnt(%0)" ::"i"(N) : "memory"); }
DI void bar_lgkm() { asm volatile("s_waitcnt lgkmcnt(0)\n\ts_barrier" ::: "memory"); }

template <int XP_, int WNT_, bool EXTRA_, int NST_>
struct GemmCfg {
  static constexpr int XP = XP_, WNT = WNT_, NST = NST_;
  static constexpr bool EXTRA = EXTRA_;
  static constexpr int WP = WNT * 2, NP = XP + WP;
  static constexpr int PLO = NP / 8, PREM = NP % 8;
  static constexpr int W_OFF = XP * 1024, E_OFF = NP * 1024;
  static constexpr int STAGE = NP * 1024 + (EXTRA ? 2048 : 0);
  static constexpr int LDS = STAGE * NST;
};

template <class C, int MTW, int NTW, class ColFn>
DI void gemm_core(const char* __restrict__ X, int row0, const char* __restrict__ W, int ldwB, ColFn colbyte,
                  const char* __restrict__ E, int ecol_dw, int rot, char* smem, int wave, int lane,
                  int mt0, int nt0, int ntcnt, bool do_extra, bool extra_swap,
                  f32x4 (&acc)[MTW][NTW], f32x4 (&eacc)[MTW]) {
  const unsigned lds0 = (unsigned)(uintptr_t)smem;
  const char* src[C::PLO + 1];
  int step[C::PLO + 1];
#pragma unroll
  for (int i = 0; i <= C::PLO; ++i) {
    int p = wave + 8 * i;
    if (p >= C::NP) p = C::NP - 1;
    if (p < C::XP) {
      int r = row0 + p * 8 + (lane >> 3);
      r = r < 0 ? 0 : (r > SEQ - 1 ? SEQ - 1 : r);
      int c = (lane & 7) ^ (lane >> 3);
      src[i] = X + (size_t)r * ROWB + c * 16;
      step[i] = 128;
    } else {
      int ci = (p - C::XP) * 64 + lane;
      int u = ci >> 4, w = ci & 15;
      int kp = u / C::WNT, nt = u - kp * C::WNT;
      int k = kp * 8 + (((w >> 3) ^ kp) & 1) * 4 + ((w >> 1) & 3);
      src[i] = W + (size_t)k * ldwB + colbyte(nt * 2 + (w & 1));
      step[i] = 64 * ldwB;
    }
  }
  const char* esrc = nullptr;
  if constexpr (C::EXTRA) {
    int w = lane >> 2, kp = wave;
    int k = kp * 8 + (((w >> 3) ^ kp) & 1) * 4 + ((w >> 1) & 3);
    int dw = ecol_dw + (w & 1) * 4 + (lane & 3);
    dw = dw > 8 ? 8 : dw;
    esrc = E + k * 36 + dw * 4;
  }
  const bool more = wave < C::PREM;
  auto issue = [&](int j) {
    int ks = j + rot; ks = ks >= 9 ? ks - 9 : ks;
    const unsigned sb = lds0 + (j % C::NST) * C::STAGE;
#pragma unroll
    for (int i = 0; i < C::PLO; ++i) glds16(src[i] + (size_t)ks * step[i], sb + (wave + 8 * i) * 1024);
    if (C::PREM && more) glds16(src[C::PLO] + (size_t)ks * step[C::PLO], sb + (wave + 8 * C::PLO) * 1024);
    if constexpr (C::EXTRA) glds4(esrc + ks * (64 * 36), sb + C::E_OFF + wave * 256);
  };
  constexpr int PE = C::EXTRA ? 1 : 0;
  const int fr = lane & 15, kg = lane >> 4;
  int xoff[2];
  xoff[0] = (mt0 * 16 + fr) * 128 + (((0 + kg) ^ (fr & 7)) << 4);
  xoff[1] = (mt0 * 16 + fr) * 128 + (((4 + kg) ^ (fr & 7)) << 4);
  const int tro = (fr >> 2) * 32 + (fr & 3) * 8;
  const int woff0 = C::W_OFF + (kg * C::WNT + nt0) * 256 + ((kg & 1) << 7) + tro;
  const int woff1 = C::W_OFF + (kg * C::WNT + nt0) * 256 + (((kg & 1) ^ 1) << 7) + tro;
  const int eoff0 = C::E_OFF + kg * 256 + ((kg & 1) << 7) + tro;
  const int eoff1 = C::E_OFF + kg * 256 + (((kg & 1) ^ 1) << 7) + tro;

#pragma unroll
  for (int j = 0; j < C::NST - 1; ++j) issue(j);
#pragma unroll
  for (int j = 0; j < 9; ++j) {
    constexpr int dummy = 0; (void)dummy;
    const int ahead = (C::NST - 2) < (8 - j) ? (C::NST - 2) : (8 - j);
    if (C::PREM && more) {
      switch (ahead) {
        case 0: wait_vm<0>(); break;
        case 1: wait_vm<1 * (C::PLO + 1 + PE)>(); break;
        case 2: wait_vm<2 * (C::PLO + 1 + PE)>(); break;
        case 3: wait_vm<3 * (C::PLO + 1 + PE)>(); break;
        default: wait_vm<4 * (C::PLO + 1 + PE)>(); break;
      }
    } else {
      switch (ahead) {
        case 0: wait_vm<0>(); break;
        case 1: wait_vm<1 * (C::PLO + PE)>(); break;
        case 2: wait_vm<2 * (C::PLO + PE)>(); break;
        case 3: wait_vm<3 * (C::PLO + PE)>(); break;
        default: wait_vm<4 * (C::PLO + PE)>(); break;
      }
    }
    bar_lgkm();
    if (j + C::NST - 1 < 9) issue(j + C::NST - 1);
    char* sb = smem + (j % C::NST) * C::STAGE;
#pragma unroll
    for (int kk = 0; kk < 2; ++kk) {
      bf16x8 xf[MTW];
#pragma unroll
      for (int m = 0; m < MTW; ++m) xf[m] = *(const bf16x8*)(sb + xoff[kk] + m * 2048);
#pragma unroll
      for (int n = 0; n < NTW; ++n) {
        if (n < ntcnt) {
          s16x4 lo = __builtin_amdgcn_ds_read_tr16_b64_v4i16((LDSP(s16x4))(sb + woff0 + (kk * 4 * C::WNT + n) * 256));
          s16x4 hi = __builtin_amdgcn_ds_read_tr16_b64_v4i16((LDSP(s16x4))(sb + woff1 + (kk * 4 * C::WNT + n) * 256));
          bf16x8 wf = __builtin_bit_cast(bf16x8, __builtin_shufflevector(lo, hi, 0, 1, 2, 3, 4, 5, 6, 7));
#pragma unroll
          for (int m = 0; m < MTW; ++m) acc[m][n] = __builtin_amdgcn_mfma_f32_16x16x32_bf16(wf, xf[m], acc[m][n], 0, 0, 0);
        }
      }
      if constexpr (C::EXTRA) {
        if (do_extra) {
          s16x4 lo = __builtin_amdgcn_ds_read_tr16_b64_v4i16((LDSP(s16x4))(sb + eoff0 + kk * 1024));
          s16x4 hi = __builtin_amdgcn_ds_read_tr16_b64_v4i16((LDSP(s16x4))(sb + eoff1 + kk * 1024));
          bf16x8 wf = __builtin_bit_cast(bf16x8, __builtin_shufflevector(lo, hi, 0, 1, 2, 3, 4, 5, 6, 7));
          if (extra_swap) {
#pragma unroll
            for (int m = 0; m < MTW; ++m) eacc[m] = __builtin_amdgcn_mfma_f32_16x16x32_bf16(wf, xf[m], eacc[m], 0, 0, 0);
          } else {
#pragma unroll
            for (int m = 0; m < MTW; ++m) eacc[m] = __builtin_amdgcn_mfma_f32_16x16x32_bf16(xf[m], wf, eacc[m], 0, 0, 0);
          }
        }
      }
    }
  }
}

using CfgA = GemmCfg<12, 14, false, 4>;
using CfgB = GemmCfg<20, 9, true, 4>;
constexpr int A_NEW = 82, A_RB = 25, A_UNITS = A_RB * 8;
constexpr int B_RB = 13, B_UNITS = B_RB * 4;
constexpr int K1_LDS = CfgA::LDS > CfgB::LDS ? CfgA::LDS : CfgB::LDS;

__global__ __launch_bounds__(512) void k1_proj(const char* __restrict__ xA, const char* __restrict__ wqkv,
                                               const char* __restrict__ xB, const char* __restrict__ wq,
                                               const char* __restrict__ wk, const char* __restrict__ wv,
                                               char* __restrict__ ws) {
  extern __shared__ __attribute__((aligned(16))) char smem[];
  const int tid = threadIdx.x, lane = tid & 63;
  const int wave = __builtin_amdgcn_readfirstlane(tid >> 6);
  const int b = blockIdx.x;
  const int unit = (b & 7) * 32 + (b >> 3);
  const int fr = lane & 15, kg = lane >> 4;
  if (unit < A_UNITS) {
    const int rb = unit >> 3, g = unit & 7;
    const int row0 = rb * A_NEW - 14;
    f32x4 acc[3][4] = {};
    f32x4 eacc[3] = {};
    const int wm = wave & 1, wn = wave >> 1;
    const int nt0 = wn < 2 ? wn * 4 : 8 + (wn - 2) * 3, ntc = wn < 2 ? 4 : 3;
    auto colbyte = [&](int ch) { ch = ch > 26 ? 26 : ch; int seg = ch / 9; return seg * ROWB + g * 144 + (ch - seg * 9) * 16; };
    gemm_core<CfgA, 3, 4>(xA, row0, wqkv, 3 * ROWB, colbyte, nullptr, 0, unit % 9, smem, wave, lane, wm * 3, nt0, ntc, false, false, acc, eacc);
    bar_lgkm();
    constexpr int QK_B = 96 * 48, V_BASE = 8 * QK_B, V_B = 96 * 80;
#pragma unroll
    for (int n = 0; n < 4; ++n) {
      if (n < ntc) {
        const int c0 = (nt0 + n) * 16 + kg * 4;
#pragma unroll
        for (int pr = 0; pr < 2; ++pr) {
          const int c = c0 + pr * 2;
          if (c < 216) {
            const int hd = c / 18, d = c - hd * 18;
#pragma unroll
            for (int m = 0; m < 3; ++m) {
              const int r = (wm * 3 + m) * 16 + fr;
              const unsigned u = pk2(acc[m][n][pr * 2], acc[m][n][pr * 2 + 1]);
              if (hd < 8) *(unsigned*)(smem + hd * QK_B + r * 48 + d * 2) = u;
              else { f32x2 f = {bf_lo(u), bf_hi(u)}; *(f32x2*)(smem + V_BASE + (hd - 8) * V_B + r * 80 + d * 4) = f; }
            }
          }
        }
      }
    }
    bar_lgkm();
    if (tid < A_NEW * 4) {
      const int hd = tid / A_NEW, i = tid - hd * A_NEW;
      const int r = 14 + i, grow = row0 + r;
      if (grow < SEQ) {
        const char* qp = smem + hd * QK_B + r * 48;
        const u32x4 q0 = *(const u32x4*)qp, q1 = *(const u32x4*)(qp + 16);
        const unsigned q2 = *(const unsigned*)(qp + 32);
        float lg[8];
        float mx = -3.0e38f;
#pragma unroll
        for (int w = 0; w < 8; ++w) {
          const char* kp = smem + (4 + hd) * QK_B + (r - 2 * w) * 48;
          const u32x4 k0 = *(const u32x4*)kp, k1 = *(const u32x4*)(kp + 16);
          const unsigned k2 = *(const unsigned*)(kp + 32);
          float s = 0.f;
#pragma unroll
          for (int e = 0; e < 4; ++e) s = dot2(q0[e], k0[e], s);
#pragma unroll
          for (int e = 0; e < 4; ++e) s = dot2(q1[e], k1[e], s);
          s = dot2(q2, k2, s);
          s *= QSCALE;
          s = (grow - 2 * w >= 0) ? s : -1.0e9f * 1.4426950408889634f;
          lg[w] = s;
          mx = fmaxf(mx, s);
        }
        float sum = 0.f;
#pragma unroll
        for (int w = 0; w < 8; ++w) { lg[w] = __builtin_amdgcn_exp2f(lg[w] - mx); sum += lg[w]; }
        const float inv = 1.0f / sum;
        float o[18];
#pragma unroll
        for (int d = 0; d < 18; ++d) o[d] = 0.f;
#pragma unroll
        for (int w = 0; w < 8; ++w) {
          const char* vp = smem + V_BASE + hd * V_B + (r - 2 * w) * 80;
          const float p = lg[w] * inv;
#pragma unroll
          for (int e = 0; e < 4; ++e) {
            const f32x4 v = *(const f32x4*)(vp + e * 16);
#pragma unroll
            for (int t = 0; t < 4; ++t) o[e * 4 + t] = fmaf(p, v[t], o[e * 4 + t]);
          }
          const f32x2 v = *(const f32x2*)(vp + 64);
          o[16] = fmaf(p, v[0], o[16]); o[17] = fmaf(p, v[1], o[17]);
        }
        unsigned* dst = (unsigned*)(ws + WS_OA + (size_t)grow * ROWB + (g * 4 + hd) * 36);
#pragma unroll
        for (int e = 0; e < 9; ++e) dst[e] = pk2(o[2 * e], o[2 * e + 1]);
      }
    }
  } else if (unit < A_UNITS + B_UNITS) {
    const int u = unit - A_UNITS, rb = u >> 2, cg = u & 3;
    const int row0 = rb * 160;
    f32x4 acc[5][3] = {};
    f32x4 eacc[5] = {};
    const int wm = wave & 1, wn = wave >> 1;
    const int nt0 = wn < 2 ? wn * 3 : 6 + (wn - 2) * 2, ntc = wn < 2 ? 3 : (wn == 2 ? 2 : 1);
    auto colbyte = [&](int ch) { return cg * 288 + ch * 16; };
    const bool isk = cg < 2;
    gemm_core<CfgB, 5, 3>(xB, row0, wq, ROWB, colbyte, isk ? wk : wv, (cg & 1) * 8, u % 9, smem, wave, lane, wm * 5, nt0, ntc, wn == 3, isk, acc, eacc);
#pragma unroll
    for (int n = 0; n < 3; ++n) {
      if (n < ntc) {
#pragma unroll
        for (int pr = 0; pr < 2; ++pr) {
          const int c = cg * 144 + (nt0 + n) * 16 + kg * 4 + pr * 2;
          const int hd = c / 18, d = c - hd * 18;
#pragma unroll
          for (int m = 0; m < 5; ++m) {
            const int r = row0 + (wm * 5 + m) * 16 + fr;
            if (r < SEQ) {
              const unsigned rq = pk2(acc[m][n][pr * 2], acc[m][n][pr * 2 + 1]);
              const unsigned uq = pk2(bf_lo(rq) * QSCALE, bf_hi(rq) * QSCALE);
              if (d < 16) *(unsigned*)(ws + WS_QM + (size_t)r * 1024 + ((d >> 3) * 32 + hd) * 16 + (d & 7) * 2) = uq;
              else *(unsigned*)(ws + WS_QR + (size_t)r * 128 + hd * 4) = uq;
            }
          }
        }
      }
    }
    if (wn == 3) {
      char* kv = ws + WS_KV;
#pragma unroll
      for (int m = 0; m < 5; ++m) {
        const int rt = row0 + (wm * 5 + m) * 16;
        if (rt < SEQ) {
          if (isk) {
            const int key = rt + fr, t = key >> 5, k32 = key & 31;
            char* ch = kv + (t >> 3) * CHUNK_B;
            const unsigned u0 = pk2(eacc[m][0], eacc[m][1]), u1 = pk2(eacc[m][2], eacc[m][3]);
            if (cg == 0) {
              u32x2 uu = {u0, u1};
              *(u32x2*)(ch + (t & 7) * 1024 + ((kg >> 1) * 32 + k32) * 16 + (kg & 1) * 8) = uu;
            } else if (kg == 0) {
              *(unsigned*)(ch + KREM_OFF + ((t >> 2) & 1) * 512 + k32 * 16 + (t & 3) * 4) = u0;
            }
          } else {
            const int key = rt + kg * 4, t = key >> 5, k32 = key & 31;
            const int s = k32 >> 4, jh = (k32 >> 3) & 1, h = (k32 >> 2) & 1;
            char* vt = kv + (t >> 3) * CHUNK_B + V_OFF + (t & 7) * VTILE_B + (s * 2 + h) * 320 + jh * 8;
            u32x2 uu = {pk2(eacc[m][0], eacc[m][1]), pk2(eacc[m][2], eacc[m][3])};
            if (cg == 2) *(u32x2*)(vt + fr * 16) = uu;
            else if (fr < 4) {
              if (fr >= 2) { uu[0] = 0x3f803f80u; uu[1] = 0x3f803f80u; }
              *(u32x2*)(vt + (16 + fr) * 16) = uu;
            }
          }
        }
      }
    }
  }
}

constexpr float M_OFF = 90.0f;

DI f32x16 qk_tile(const bf16x8 kf, unsigned kr, const bf16x8 qf, const s16x4 qr) {
  u32x2 a = {kr, 0x3f803f80u};
  f32x16 z = {};
  f32x16 s = __builtin_amdgcn_mfma_f32_32x32x8bf16_1k(__builtin_bit_cast(s16x4, a), qr, z, 0, 0, 0);
  return __builtin_amdgcn_mfma_f32_32x32x16_bf16(kf, qf, s, 0, 0, 0);
}
DI float max16(const f32x16& s, float m) {
#pragma unroll
  for (int i = 0; i < 16; i += 2) m = __builtin_fmaxf(__builtin_fmaxf(s[i], s[i + 1]), m);
  return m;
}
DI void softmax_pv(const f32x16& s, const bf16x8 v0, const bf16x8 v1, f32x16& o) {
  u32x4 p0, p1;
#pragma unroll
  for (int i = 0; i < 4; ++i) p0[i] = pk2(__builtin_amdgcn_exp2f(s[2 * i]), __builtin_amdgcn_exp2f(s[2 * i + 1]));
  o = __builtin_amdgcn_mfma_f32_32x32x16_bf16(v0, __builtin_bit_cast(bf16x8, p0), o, 0, 0, 0);
#pragma unroll
  for (int i = 0; i < 4; ++i) p1[i] = pk2(__builtin_amdgcn_exp2f(s[8 + 2 * i]), __builtin_amdgcn_exp2f(s[8 + 2 * i + 1]));
  o = __builtin_amdgcn_mfma_f32_32x32x16_bf16(v1, __builtin_bit_cast(bf16x8, p1), o, 0, 0, 0);
}

DI void attn_chunk(const char* cb, int koff, int kroff, int voff, const bf16x8 qf, const s16x4 qr, f32x16& o) {
#pragma unroll
  for (int g = 0; g < 2; ++g) {
    const u32x4 kr = *(const u32x4*)(cb + KREM_OFF + g * 512 + kroff);
#pragma unroll
    for (int t = 0; t < 4; ++t) {
      const int tt = g * 4 + t;
      const bf16x8 kf = *(const bf16x8*)(cb + tt * 1024 + koff);
      const bf16x8 v0 = *(const bf16x8*)(cb + V_OFF + tt * VTILE_B + voff);
      const bf16x8 v1 = *(const bf16x8*)(cb + V_OFF + tt * VTILE_B + 640 + voff);
      const f32x16 s = qk_tile(kf, kr[t], qf, qr);
      softmax_pv(s, v0, v1, o);
    }
  }
}
DI float max_chunk(const char* cb, int koff, int kroff, const bf16x8 qf, const s16x4 qr, float m, int ntiles) {
  for (int g = 0; g < 2; ++g) {
    const u32x4 kr = *(const u32x4*)(cb + KREM_OFF + g * 512 + kroff);
#pragma unroll
    for (int t = 0; t < 4; ++t) {
      const int tt = g * 4 + t;
      if (tt < ntiles) {
        const bf16x8 kf = *(const bf16x8*)(cb + tt * 1024 + koff);
        m = max16(qk_tile(kf, kr[t], qf, qr), m);
      }
    }
  }
  return m;
}
DI s16x4 make_qr(unsigned qrem, float m, int h) {
  const unsigned hi = pk2(-m, 0.f) & 0xffffu;
  const float rem = -m - bf_lo(hi);
  const unsigned lo = pk2(rem, 0.f) & 0xffffu;
  u32x2 r = {qrem, hi | (lo << 16)};
  if (h) { r[0] = 0; r[1] = 0; }
  return __builtin_bit_cast(s16x4, r);
}

__global__ __launch_bounds__(512) void k2_attn(char* __restrict__ ws) {
  extern __shared__ __attribute__((aligned(16))) char smem[];
  const int tid = threadIdx.x, lane = tid & 63;
  const int wave = __builtin_amdgcn_readfirstlane(tid >> 6);
  const int row = blockIdx.x * 8 + wave;
  const int c32 = lane & 31, h = lane >> 5;
  const unsigned lds0 = (unsigned)(uintptr_t)smem;
  bf16x8 qf;
  unsigned qrem;
  asm volatile("global_load_dwordx4 %0, %1, off" : "=v"(qf) : "v"(ws + WS_QM + (size_t)row * 1024 + lane * 16) : "memory");
  asm volatile("global_load_dword %0, %1, off" : "=v"(qrem) : "v"(ws + WS_QR + (size_t)row * 128 + c32 * 4) : "memory");
  {
    const char* src = ws + WS_KV + wave * 1024 + lane * 16;
#pragma unroll
    for (int i = 0; i < 19; ++i) glds16(src + i * 8192, lds0 + (wave + 8 * i) * 1024);
  }
  const int koff = lane * 16, kroff = c32 * 16;
  const int vrow = c32 < 18 ? c32 : ((c32 >= 20 && c32 < 28) ? 0 : 18);
  const int voff = h * 320 + vrow * 16;

  asm volatile("s_waitcnt vmcnt(16)" : "+v"(qf), "+v"(qrem)::"memory");
  bar_lgkm();
  float m = max_chunk(smem, koff, kroff, qf, make_qr(qrem, 0.f, h), -3.0e38f, 2);
  m = fmaxf(m, __shfl_xor(m, 32));
  s16x4 qr = make_qr(qrem, bf_lo(pk2(m + M_OFF, 0.f)), h);
  f32x16 o = {};
  attn_chunk(smem, koff, kroff, voff, qf, qr, o);
  wait_vm<11>();
  bar_lgkm();
  for (int c = 1; c < 3; ++c) attn_chunk(smem + c * CHUNK_B, koff, kroff, voff, qf, qr, o);
  wait_vm<0>();
  bar_lgkm();
  for (int c = 3; c < 8; ++c) attn_chunk(smem + c * CHUNK_B, koff, kroff, voff, qf, qr, o);

  float l = h ? o[12] : o[10];
  float big = l;
#pragma unroll
  for (int i = 0; i < 10; ++i) big = fmaxf(big, fabsf(o[i]));
  bool ok = (big < 1.0e37f) && (l > 0.f);
#pragma unroll
  for (int i = 0; i < 10; ++i) ok = ok && (o[i] == o[i]);
  if (!__all(ok)) {
    const s16x4 qz = make_qr(qrem, 0.f, h);
    float mt = -3.0e38f;
    for (int c = 0; c < 8; ++c) mt = max_chunk(smem + c * CHUNK_B, koff, kroff, qf, qz, mt, 8);
    mt = fmaxf(mt, __shfl_xor(mt, 32));
    qr = make_qr(qrem, mt, h);
    o = f32x16{};
    for (int c = 0; c < 8; ++c) attn_chunk(smem + c * CHUNK_B, koff, kroff, voff, qf, qr, o);
    l = h ? o[12] : o[10];
  }
  const float inv = 1.0f / l;
  unsigned* dst = (unsigned*)(ws + WS_OB + (size_t)row * ROWB + c32 * 36 + h * 8);
  dst[0] = pk2(o[0] * inv, o[1] * inv);
  dst[1] = pk2(o[2] * inv, o[3] * inv);
  dst[4] = pk2(o[4] * inv, o[5] * inv);
  dst[5] = pk2(o[6] * inv, o[7] * inv);
  if (!h) dst[8] = pk2(o[8] * inv, o[9] * inv);
}

using CfgO = GemmCfg<8, 9, false, 6>;
__global__ __launch_bounds__(512) void k3_out(const char* __restrict__ ws, const char* __restrict__ woA,
                                              const char* __restrict__ woB, char* __restrict__ out) {
  extern __shared__ __attribute__((aligned(16))) char smem[];
  const int tid = threadIdx.x, lane = tid & 63;
  const int wave = __builtin_amdgcn_readfirstlane(tid >> 6);
  const int b = blockIdx.x;
  const int unit = (b & 7) * 32 + (b >> 3);
  const int br = unit >> 7, rb = (unit >> 2) & 31, cg = unit & 3;
  const int fr = lane & 15, kg = lane >> 4;
  f32x4 acc[1][5] = {};
  f32x4 eacc[1] = {};
  const int wm = wave & 3, wn = wave >> 2;
  const int nt0 = wn * 5, ntc = wn ? 4 : 5;
  auto colbyte = [&](int ch) { return cg * 288 + ch * 16; };
  gemm_core<CfgO, 1, 5>(ws + (br ? WS_OB : WS_OA), rb * 64, br ? woB : woA, ROWB, colbyte, nullptr, 0, unit % 9, smem, wave, lane, wm, nt0, ntc, false, false, acc, eacc);
  char* dst = out + (size_t)(rb * 64 + wm * 16 + fr) * (2 * ROWB) + br * ROWB + cg * 288 + nt0 * 32 + kg * 8;
#pragma unroll
  for (int n = 0; n < 5; ++n) {
    if (n < ntc) {
      u32x2 uu = {pk2(acc[0][n][0], acc[0][n][1]), pk2(acc[0][n][2], acc[0][n][3])};
      *(u32x2*)(dst + n * 32) = uu;
    }
  }
}

extern "C" void kernel_launch(void* const* d_in, const int* in_sizes, int n_in, void* d_out, int out_size,
                              void* d_ws, size_t ws_size, hipStream_t stream) {
  (void)in_sizes; (void)n_in; (void)out_size; (void)ws_size;
  (void)hipFuncSetAttribute((const void*)k1_proj, hipFuncAttributeMaxDynamicSharedMemorySize, K1_LDS);
  (void)hipFuncSetAttribute((const void*)k2_attn, hipFuncAttributeMaxDynamicSharedMemorySize, KV_BYTES);
  (void)hipFuncSetAttribute((const void*)k3_out, hipFuncAttributeMaxDynamicSharedMemorySize, CfgO::LDS);
  char* ws = (char*)d_ws;
  k1_proj<<<256, 512, K1_LDS, stream>>>((const char*)d_in[0], (const char*)d_in[1], (const char*)d_in[3],
                                        (const char*)d_in[4], (const char*)d_in[5], (const char*)d_in[6], ws);
  k2_attn<<<256, 512, KV_BYTES, stream>>>(ws);
  k3_out<<<256, 512, CfgO::LDS, stream>>>(ws, (const char*)d_in[2], (const char*)d_in[7], (char*)d_out);
}
```

```cpp
#include <hip/hip_runtime.h>
#include <hip/hip_bf16.h>
#include <stdint.h>

typedef __bf16 bf16x2 __attribute__((ext_vector_type(2)));
typedef __bf16 bf16x8 __attribute__((ext_vector_type(8)));
typedef short s16x4 __attribute__((ext_vector_type(4)));
typedef float f32x2 __attribute__((ext_vector_type(2)));
typedef float f32x4 __attribute__((ext_vector_type(4)));
typedef float f32x16 __attribute__((ext_vector_type(16)));
typedef unsigned u32x2 __attribute__((ext_vector_type(2)));
typedef unsigned u32x4 __attribute__((ext_vector_type(4)));
#define LDSP(T) __attribute__((address_space(3))) T*
#define DI __device__ __forceinline__
#define STAMP_DECL
#define STAMP(i) do {} while (0)
#define CSTAMP(i) do {} while (0)
#define LSTAMP(i) do {} while (0)
#define LSTAMP_OUT(w) do {} while (0)
#define STAMP_ARG
#define STAMP_PASS
#define STAMP_OUT(kid, wsp) do {} while (0)

#define K1UNROLL _Pragma("unroll")
#define PRIO(g) ((g) < 0 ? -1 : 3 - (((g) >> 1) & 3))
constexpr int SEQ = 2048, DM = 576;
constexpr int ROWB = DM * 2;
constexpr float LOG2E = 1.4426950408889634f;
constexpr float QSCALE = 0.23570226039551584f * LOG2E;

constexpr int GRP_B = 9728, KR_OFF = 4096, V_OFF = 4608, VT_B = 1280;
constexpr int KV_BYTES = 16 * GRP_B;
constexpr size_t WS_KV = 0;
constexpr size_t WS_QB = WS_KV + KV_BYTES;
constexpr size_t WS_QKV = WS_QB + (size_t)SEQ * ROWB;
constexpr size_t WS_OA = WS_QKV + (size_t)SEQ * 3 * ROWB;
constexpr size_t WS_OB = WS_OA + (size_t)SEQ * ROWB;
constexpr size_t WS_WF = WS_OB + (size_t)SEQ * ROWB;
constexpr size_t WS_CNT = WS_WF + (size_t)72 * 18432;

template <int N> struct IC { static constexpr int value = N; };
DI unsigned pk2(float a, float b) {
  f32x2 f = {a, b};
  return __builtin_bit_cast(unsigned, __builtin_convertvector(f, bf16x2));
}
DI float dot2(unsigned a, unsigned b, float c) {
  return __builtin_amdgcn_fdot2_f32_bf16(__builtin_bit_cast(bf16x2, a), __builtin_bit_cast(bf16x2, b), c, false);
}
DI float bf_lo(unsigned u) { return __uint_as_float(u << 16); }
DI float bf_hi(unsigned u) { return __uint_as_float(u & 0xffff0000u); }

DI void glds16(unsigned voff, const void* sbase, unsigned lds_dst) {
  unsigned keep;
  asm volatile("s_mov_b32 %0, m0\n\ts_mov_b32 m0, %3\n\ts_nop 0\n\tglobal_load_lds_dwordx4 %1, %2\n\ts_mov_b32 m0, %0"
               : "=&s"(keep) : "v"(voff), "s"(sbase), "s"(lds_dst) : "memory");
}
DI void glds4(unsigned voff, const void* sbase, unsigned lds_dst) {
  unsigned keep;
  asm volatile("s_mov_b32 %0, m0\n\ts_mov_b32 m0, %3\n\ts_nop 0\n\tglobal_load_lds_dword %1, %2\n\ts_mov_b32 m0, %0"
               : "=&s"(keep) : "v"(voff), "s"(sbase), "s"(lds_dst) : "memory");
}
template <int N> DI void wait_vm() { asm volatile("s_waitcnt vmcnt(%0)" ::"i"(N) : "memory"); }
DI void bar_lgkm() { asm volatile("s_waitcnt lgkmcnt(0)\n\ts_barrier" ::: "memory"); }
DI bf16x8 tr_frag(const char* p) {
  s16x4 lo = __builtin_amdgcn_ds_read_tr16_b64_v4i16((LDSP(s16x4))(p));
  s16x4 hi = __builtin_amdgcn_ds_read_tr16_b64_v4i16((LDSP(s16x4))(p + 256));
  return __builtin_bit_cast(bf16x8, __builtin_shufflevector(lo, hi, 0, 1, 2, 3, 4, 5, 6, 7));
}
#define ST16(p, v) __builtin_nontemporal_store(v, (u32x4*)(p))
#define MFMA16(a, b, c) __builtin_amdgcn_mfma_f32_16x16x32_bf16(a, b, c, 0, 0, 0)

constexpr int K1_XB = 20480, K1_STAGE = 36864, K1_LDS = 4 * K1_STAGE;
constexpr int K1_RB = 13, K1_ACT = 14, K1_BCT = 5, K1_GEMM = K1_RB * (K1_ACT + K1_BCT);

template <int MODE>
DI void k1_loader(const char* X, int row0, const char* W0, const char* W1, int ldw, int colB0, int colB1,
                  const char* wk, const char* wv, int rot, unsigned lds0, int l, int lane) {
  unsigned xo[5], wo[4], eo[8];
  if (MODE != 2) {
#pragma unroll
    for (int i = 0; i < 5; ++i) {
      int r = row0 + (l + 4 * i) * 8 + (lane >> 3);
      r = r > SEQ - 1 ? SEQ - 1 : r;
      xo[i] = r * ROWB + (((lane & 7) ^ (lane >> 3)) << 4);
    }
  }
#pragma unroll
  for (int i = 0; i < 4; ++i) {
    const int kp = l + 4 * (i >> 1);
    const int k = kp * 8 + ((lane >> 2) & 7);
    wo[i] = k * ldw + ((i & 1) ? colB1 : colB0) + (lane >> 5) * 64 + (((lane & 3) ^ ((kp & 1) << 1)) << 4);
  }
  if (MODE == 1) {
#pragma unroll
    for (int i = 0; i < 4; ++i) {
      const int kp = l + 4 * (i >> 1);
      const int k = kp * 8 + (i & 1) * 4 + (lane >> 4);
      int dw = (lane & 15) ^ ((kp & 1) << 3);
      dw = dw > 8 ? 8 : dw;
      eo[i] = k * 36 + dw * 4;
    }
  }
  auto issue = [&](int j) {
    int ks = j + rot; ks = ks >= 9 ? ks - 9 : ks;
    const unsigned sb = lds0 + (j & 3) * K1_STAGE;
    if (MODE != 2) {
      const char* xb = X + ks * 128;
#pragma unroll
      for (int i = 0; i < 5; ++i) glds16(xo[i], xb, sb + (l + 4 * i) * 1024);
    }
    const char* w0 = W0 + (size_t)ks * 64 * ldw;
    const char* w1 = W1 + (size_t)ks * 64 * ldw;
#pragma unroll
    for (int i = 0; i < 4; ++i) {
      if (MODE == 1 && (i & 1)) continue;
      glds16(wo[i], (i & 1) ? w1 : w0, sb + K1_XB + (l + 4 * (i >> 1)) * 2048 + (i & 1) * 1024);
    }
    if (MODE == 1) {
      const char* kb = wk + ks * (64 * 36);
      const char* vb = wv + ks * (64 * 36);
#pragma unroll
      for (int i = 0; i < 4; ++i) {
        const unsigned d = sb + K1_XB + (l + 4 * (i >> 1)) * 2048 + 1024 + (i & 1) * 256;
        glds4(eo[i], kb, d);
        glds4(eo[i], vb, d + 512);
      }
    }
  };
  constexpr int P = MODE == 0 ? 9 : (MODE == 1 ? 15 : 4);
#pragma unroll 1
  for (int t = 0; t < 9; ++t) {
    issue(t);
    if (t == 1) { wait_vm<P>(); bar_lgkm(); }
    else if (t >= 3) { wait_vm<2 * P>(); bar_lgkm(); }
  }
  wait_vm<P>(); bar_lgkm();
  wait_vm<0>(); bar_lgkm();
  bar_lgkm();
}

__global__ __launch_bounds__(512) void k1_proj(const char* __restrict__ xA, const char* __restrict__ wqkv,
                                               const char* __restrict__ xB, const char* __restrict__ wq,
                                               const char* __restrict__ wk, const char* __restrict__ wv,
                                               const char* __restrict__ woA, const char* __restrict__ woB,
                                               char* __restrict__ ws) {
  extern __shared__ __attribute__((aligned(16))) char smem[];
  const int tid = threadIdx.x, lane = tid & 63;
  const int wave = __builtin_amdgcn_readfirstlane(tid >> 6);
  STAMP_DECL; STAMP(0);
  const unsigned lds0 = (unsigned)(uintptr_t)smem;
  const int b = blockIdx.x;
  const int unit = (b & 7) * 32 + (b >> 3);
  const int fr = lane & 15, kg = lane >> 4;
  const int rot = unit % 9;
  const int tro = kg * 2048 + (fr >> 2) * 64 + (fr & 3) * 8;
  const int trE = K1_XB + tro + ((kg & 1) << 5), trO = K1_XB + tro + (((kg & 1) ^ 1) << 5);

  if (unit >= K1_GEMM) {
    const int s = unit - K1_GEMM;
    if (s == 0 && tid < 256) *(unsigned*)(ws + WS_CNT + tid * 128) = 0u;
    if (wave >= 4) {
      const int c0 = s * 128, c1 = c0 + 64;
      k1_loader<2>(nullptr, 0, c0 < DM ? woA : woB, c1 < DM ? woA : woB, ROWB, (c0 < DM ? c0 : c0 - DM) * 2, (c1 < DM ? c1 : c1 - DM) * 2,
                   nullptr, nullptr, rot, lds0, wave - 4, lane);
    } else {
      bar_lgkm();
      char* dst = ws + WS_WF + (size_t)(s * 8 + wave * 2) * 18432 + lane * 16;
#pragma unroll 1
      for (int j = 0; j < 9; ++j) {
        int ks = j + rot; ks = ks >= 9 ? ks - 9 : ks;
        const char* sb = smem + (j & 3) * K1_STAGE + wave * 512;
        const bf16x8 f0 = tr_frag(sb + trE), f1 = tr_frag(sb + trO), f2 = tr_frag(sb + trE + 8192), f3 = tr_frag(sb + trO + 8192);
        bar_lgkm();
        ST16(dst + ks * 2048, __builtin_bit_cast(u32x4, f0));
        ST16(dst + ks * 2048 + 18432, __builtin_bit_cast(u32x4, f1));
        ST16(dst + ks * 2048 + 1024, __builtin_bit_cast(u32x4, f2));
        ST16(dst + ks * 2048 + 18432 + 1024, __builtin_bit_cast(u32x4, f3));
      }
    }
    STAMP(9);
    STAMP_OUT(0, ws);
    return;
  }
  const bool isA = unit < K1_RB * K1_ACT;
  int rb, ct;
  if (isA) {
    if (unit < 70) { ct = unit / 5; rb = unit - ct * 5; }
    else { const int u = unit - 70, band = u >= 56 ? 1 : 0, v = u - band * 56; ct = v >> 2; rb = 5 + band * 4 + (v & 3); }
  } else { const int u = unit - K1_RB * K1_ACT; rb = u / K1_BCT; ct = u - rb * K1_BCT; }
  const int row0 = rb * 160;
  const bool last = ct == (isA ? K1_ACT - 1 : K1_BCT - 1);
  const int wm = wave & 1, wn = (wave >> 1) & 1;
  constexpr int OT_LD = 272;
  if (wave >= 4) {
    if (isA) k1_loader<0>(xA, row0, wqkv, wqkv, 3 * ROWB, ct * 256, ct * 256 + (last ? 0 : 128), nullptr, nullptr, rot, lds0, wave - 4, lane);
    else if (!last) k1_loader<0>(xB, row0, wq, wq, ROWB, ct * 256, ct * 256 + 128, nullptr, nullptr, rot, lds0, wave - 4, lane);
    else k1_loader<1>(xB, row0, wq, wq, ROWB, ct * 256, 0, wk, wv, rot, lds0, wave - 4, lane);
  } else {
    f32x4 acc[5][4] = {};
    const int xo0 = (wm * 80 + fr) * 128 + ((kg ^ (fr & 7)) << 4), xo1 = xo0 ^ 64;
    const int wE = trE + wn * 1024, wO = trO + wn * 1024;
    bf16x8 xa[5], wa[4], xb[5], wb[4];
    auto rd = [&](const char* sb, int kk, bf16x8 (&xf)[5], bf16x8 (&wf)[4]) {
#pragma unroll
      for (int m = 0; m < 5; ++m) xf[m] = *(const bf16x8*)(sb + (kk ? xo1 : xo0) + m * 2048);
#pragma unroll
      for (int n = 0; n < 4; ++n) wf[n] = tr_frag(sb + ((n & 1) ? wO : wE) + kk * 8192 + (n >> 1) * 512);
    };
    auto mm = [&](bf16x8 (&xf)[5], bf16x8 (&wf)[4]) {
#pragma unroll
      for (int n = 0; n < 4; ++n)
#pragma unroll
        for (int m = 0; m < 5; ++m) acc[m][n] = MFMA16(wf[n], xf[m], acc[m][n]);
    };
    bar_lgkm();
    STAMP(1); CSTAMP(10);
    rd(smem, 0, xa, wa);
#define ILV() do { _Pragma("unroll") for (int q_ = 0; q_ < 13; ++q_) { __builtin_amdgcn_sched_group_barrier(0x8, 1, 0); __builtin_amdgcn_sched_group_barrier(0x100, 1, 0); } \
                   __builtin_amdgcn_sched_group_barrier(0x8, 7, 0); } while (0)
    K1UNROLL
    for (int j = 0; j < 9; ++j) {
      rd(smem + (j & 3) * K1_STAGE, 1, xb, wb);
      mm(xa, wa);
      ILV();
      __builtin_amdgcn_sched_barrier(0);
      bar_lgkm();
      rd(smem + ((j + 1) & 3) * K1_STAGE, 0, xa, wa);
      mm(xb, wb);
      ILV();
      __builtin_amdgcn_sched_barrier(0);
    }
    STAMP(2); CSTAMP(11);
    const bool scale = !isA && !(last && wn == 1);
#pragma unroll
    for (int m = 0; m < 5; ++m)
#pragma unroll
      for (int n = 0; n < 4; ++n) {
        u32x2 u = {pk2(acc[m][n][0], acc[m][n][1]), pk2(acc[m][n][2], acc[m][n][3])};
        if (scale) {
          u[0] = pk2(bf_lo(u[0]) * QSCALE, bf_hi(u[0]) * QSCALE);
          u[1] = pk2(bf_lo(u[1]) * QSCALE, bf_hi(u[1]) * QSCALE);
        }
        *(u32x2*)(smem + (wm * 80 + m * 16 + fr) * OT_LD + wn * 128 + n * 32 + kg * 8) = u;
      }
  }
  bar_lgkm();
  STAMP(3);
  {
    char* dst = isA ? ws + WS_QKV : ws + WS_QB;
    const int ld = isA ? 3 * ROWB : ROWB;
    const int c = tid & 15;
    if (!(last && c >= 8)) {
#pragma unroll
      for (int i = 0; i < 5; ++i) {
        const int r = (tid >> 4) + 32 * i;
        if (row0 + r < SEQ) ST16(dst + (size_t)(row0 + r) * ld + ct * 256 + c * 16, *(const u32x4*)(smem + r * OT_LD + c * 16));
      }
    }
  }
  if (!isA && last) {
    char* kv = ws + WS_KV;
    if (tid < 320) {
      const int r = tid >> 1, hh = tid & 1, key = row0 + r;
      if (key < SEQ) {
        const int t = key >> 5, k32 = key & 31;
        char* g = kv + (t >> 2) * GRP_B;
        *(u32x4*)(g + (t & 3) * 1024 + (hh * 32 + k32) * 16) = *(const u32x4*)(smem + r * OT_LD + 128 + hh * 16);
        if (hh == 0) *(unsigned*)(g + KR_OFF + k32 * 16 + (t & 3) * 4) = *(const unsigned*)(smem + r * OT_LD + 160);
      }
    }
    if (tid < 400) {
      const int blk = tid / 20, d = tid - blk * 20;
      const int tl = blk >> 2, s = (blk >> 1) & 1, hh = blk & 1;
      const int t = (row0 >> 5) + tl;
      if (t < 64) {
        u32x4 o;
        if (d < 18) {
          const char* src = smem + (tl * 32 + s * 16 + hh * 4) * OT_LD + 192 + d * 2;
#pragma unroll
          for (int e = 0; e < 4; ++e) {
            const int r = (e >> 1) * 8 + (e & 1) * 2;
            o[e] = (unsigned)*(const unsigned short*)(src + r * OT_LD) | ((unsigned)*(const unsigned short*)(src + (r + 1) * OT_LD) << 16);
          }
        } else {
          const unsigned f = d == 18 ? 0x3f803f80u : 0u;
          o = u32x4{f, f, f, f};
        }
        *(u32x4*)(kv + (t >> 2) * GRP_B + V_OFF + (t & 3) * VT_B + (s * 2 + hh) * 320 + d * 16) = o;
      }
    }
  }
  STAMP(9);
  STAMP_OUT(0, ws);
}

constexpr int K2_LDS = KV_BYTES + 6144;
constexpr float M_OFF = 96.0f;
#define SB __builtin_amdgcn_sched_barrier(0)
#define MF16(a, b, c) __builtin_amdgcn_mfma_f32_32x32x16_bf16(a, b, c, 0, 0, 0)
#define MF8(a, b, c) __builtin_amdgcn_mfma_f32_32x32x8bf16_1k(a, b, c, 0, 0, 0)
#define EX(i) cur[i] = __builtin_amdgcn_exp2f(cur[i])

DI float xhalf(float x);
DI float max16(const f32x16& s, float m) {
#pragma unroll
  for (int i = 0; i < 16; i += 2) m = __builtin_fmaxf(__builtin_fmaxf(s[i], s[i + 1]), m);
  return m;
}
#define E(i) EX(i); SB;
#define C0(i) p0[i] = pk2(cur[2 * i], cur[2 * i + 1]); SB;
#define C1(i) p1[i] = pk2(cur[8 + 2 * i], cur[9 + 2 * i]); SB;
template <bool RELOAD>
DI void phase(f32x16& cur, f32x16& nxt, s16x4& kra, const char* krp, const s16x4 qr, bf16x8& kf, const char* kp2, const bf16x8 qf,
              bf16x8& v0, const char* v0p, bf16x8& v1, const char* v1p, u32x4& p0, u32x4& p1, f32x16& o) {
  const f32x16 z = {};
  nxt = MF8(kra, qr, z); SB;
  if (RELOAD) { kra = *(const s16x4*)krp; SB; }
  E(0) E(1) E(2) E(3) C0(0) E(4)
  o = MF16(v1, __builtin_bit_cast(bf16x8, p1), o); SB;
  v1 = *(const bf16x8*)v1p; SB;
  C0(1) E(5) E(6) E(7) C0(2) E(8)
  nxt = MF16(kf, qf, nxt); SB;
  kf = *(const bf16x8*)kp2; SB;
  C0(3) E(9) E(10) E(11) C1(0) E(12)
  o = MF16(v0, __builtin_bit_cast(bf16x8, p0), o); SB;
  v0 = *(const bf16x8*)v0p; SB;
  C1(1) E(13) E(14) E(15) C1(2) C1(3)
}
DI unsigned rem_word(unsigned qrem, float m, int h) {
  const unsigned hi = pk2(-m, 0.f) & 0xffffu;
  const unsigned lo = pk2(-m - bf_lo(hi), 0.f) & 0xffffu;
  return h ? (hi | (lo << 16)) : qrem;
}
DI s16x4 mk2(unsigned a, unsigned b) { u32x2 r = {a, b}; return __builtin_bit_cast(s16x4, r); }

DI float max_pass(const char* kp, const char* krp, const bf16x8 qf, unsigned rw, int npair) {
  const s16x4 qe = mk2(rw, 0u), qo = mk2(0u, rw);
  const f32x16 z = {};
  float m = -3.0e38f;
  for (int i = 0; i < npair; ++i) {
    const int G = (i >> 1) * GRP_B, hf = i & 1;
    const s16x4 kr = *(const s16x4*)(krp + G + hf * 8);
    m = max16(MF16(*(const bf16x8*)(kp + G + hf * 2048), qf, MF8(kr, qe, z)), m);
    m = max16(MF16(*(const bf16x8*)(kp + G + hf * 2048 + 1024), qf, MF8(kr, qo, z)), m);
  }
  return fmaxf(m, xhalf(m));
}
DI void attn_exact(const char* img, int ko, int kro, int vo, const bf16x8 qf, unsigned qrem, int h, f32x16& o) {
  const float mt = max_pass(img + ko, img + kro, qf, rem_word(qrem, 0.f, h), 32);
  const unsigned rw = rem_word(qrem, mt, h);
  const s16x4 qe = mk2(rw, 0u), qo = mk2(0u, rw);
  const f32x16 z = {};
  o = z;
  for (int t = 0; t < 64; ++t) {
    const char* g = img + (t >> 2) * GRP_B;
    const s16x4 kr = *(const s16x4*)(g + kro + (t & 2) * 4);
    f32x16 cur = MF16(*(const bf16x8*)(g + ko + (t & 3) * 1024), qf, MF8(kr, (t & 1) ? qo : qe, z));
    u32x4 p0, p1;
#pragma unroll
    for (int i = 0; i < 16; ++i) EX(i);
#pragma unroll
    for (int i = 0; i < 4; ++i) { p0[i] = pk2(cur[2 * i], cur[2 * i + 1]); p1[i] = pk2(cur[8 + 2 * i], cur[9 + 2 * i]); }
    o = MF16(*(const bf16x8*)(g + vo + (t & 3) * VT_B), __builtin_bit_cast(bf16x8, p0), o);
    o = MF16(*(const bf16x8*)(g + vo + (t & 3) * VT_B + 640), __builtin_bit_cast(bf16x8, p1), o);
  }
}

constexpr int LA_0 = 98304;
constexpr int LA_Q = LA_0 + 22 * 2304;
constexpr int L_OB = 94208;
constexpr int L_P = 104448;
constexpr int P_LD = 272, P_HALF = 80 * P_LD;
constexpr int XC_B = 10240;
constexpr int L_CTL = K2_LDS;
constexpr int K2_LDS_ALL = K2_LDS + 64;
enum { C_KVA = 0, C_KVB = 4, C_PAST = 8, C_AROWS = 12, C_A2 = 16, C_XA = 20, C_DONE = 24, C_PROG = 32 };

DI void lds_add(unsigned addr) {
  asm volatile("s_waitcnt lgkmcnt(0)\n\ts_mov_b64 exec, 1\n\tds_add_u32 %0, %1\n\ts_mov_b64 exec, -1" ::"v"(addr), "v"(1u) : "memory");
}
DI void lds_wait(unsigned addr, unsigned n) {
  for (;;) {
    unsigned r;
    asm volatile("ds_read_b32 %0, %1\n\ts_waitcnt lgkmcnt(0)" : "=&v"(r) : "v"(addr) : "memory");
    if (__builtin_amdgcn_readfirstlane(r) >= n) break;
    __builtin_amdgcn_s_sleep(2);
  }
}
DI void cnt_wait(const char* p) {
  for (int spin = 0; spin < (1 << 22); ++spin) {
    unsigned r;
    asm volatile("global_load_dword %0, %1, off sc1\n\ts_waitcnt vmcnt(0)" : "=&v"(r) : "v"(p) : "memory");
    if (__all(r >= 16u)) break;
    __builtin_amdgcn_s_sleep(2);
  }
}
DI float xhalf(float x) {
  const unsigned u = __float_as_uint(x);
  const auto r = __builtin_amdgcn_permlane32_swap(u, u, false, false);
  return __uint_as_float((threadIdx.x & 32) ? r[0] : r[1]);
}
DI void local_attn(char* smem, unsigned lds0, int wave, int c32, int h, int row) {
  unsigned* qp = (unsigned*)(smem + LA_Q + wave * ROWB + c32 * 36);
  unsigned q[9];
#pragma unroll
  for (int e = 0; e < 9; ++e) q[e] = qp[e];
  const unsigned kb = lds0 + LA_0 + (wave + 8 - h * 8) * 2304 + c32 * 36;
  const unsigned vb = lds0 + LA_0 + wave * 2304 + ROWB + c32 * 36 + h * 16;
  u32x2 kk[9][2], vv[5][4];
#pragma unroll
  for (int e = 0; e < 9; ++e)
    asm volatile("ds_read2st64_b32 %0, %2 offset0:54 offset1:36\n\tds_read2st64_b32 %1, %2 offset0:18 offset1:0"
                 : "=&v"(kk[e][0]), "=&v"(kk[e][1]) : "v"(kb + e * 4) : "memory");
  asm volatile("s_waitcnt lgkmcnt(0)" : "+v"(kk[0][0]), "+v"(kk[0][1]), "+v"(kk[1][0]), "+v"(kk[1][1]), "+v"(kk[2][0]), "+v"(kk[2][1]),
      "+v"(kk[3][0]), "+v"(kk[3][1]), "+v"(kk[4][0]), "+v"(kk[4][1]), "+v"(kk[5][0]), "+v"(kk[5][1]), "+v"(kk[6][0]), "+v"(kk[6][1]),
      "+v"(kk[7][0]), "+v"(kk[7][1]), "+v"(kk[8][0]), "+v"(kk[8][1])::"memory");
#pragma unroll
  for (int e = 0; e < 5; ++e)
    asm volatile("ds_read2st64_b32 %0, %4 offset0:126 offset1:108\n\tds_read2st64_b32 %1, %4 offset0:90 offset1:72\n\t"
                 "ds_read2st64_b32 %2, %4 offset0:54 offset1:36\n\tds_read2st64_b32 %3, %4 offset0:18 offset1:0"
                 : "=&v"(vv[e][0]), "=&v"(vv[e][1]), "=&v"(vv[e][2]), "=&v"(vv[e][3]) : "v"(vb + e * 4) : "memory");
  float lg[4] = {0.f, 0.f, 0.f, 0.f};
#pragma unroll
  for (int e = 0; e < 9; ++e) {
    const unsigned k0 = kk[e][0][0], k1 = kk[e][0][1], k2 = kk[e][1][0], k3 = kk[e][1][1];
    lg[0] = dot2(q[e], k0, lg[0]); lg[1] = dot2(q[e], k1, lg[1]); lg[2] = dot2(q[e], k2, lg[2]); lg[3] = dot2(q[e], k3, lg[3]);
  }
#pragma unroll
  for (int w = 0; w < 4; ++w) lg[w] = row - 2 * (h * 4 + w) >= 0 ? lg[w] * QSCALE : -1.0e9f * LOG2E;
  float mx = fmaxf(fmaxf(lg[0], lg[1]), fmaxf(lg[2], lg[3]));
  mx = fmaxf(mx, xhalf(mx));
  float sum = 0.f;
#pragma unroll
  for (int w = 0; w < 4; ++w) { lg[w] = __builtin_amdgcn_exp2f(lg[w] - mx); sum += lg[w]; }
  sum += xhalf(sum);
  const float inv = __builtin_amdgcn_rcpf(sum);
  const unsigned pa = pk2(lg[0] * inv, lg[1] * inv), pb = pk2(lg[2] * inv, lg[3] * inv);
  const auto ra = __builtin_amdgcn_permlane32_swap(pa, pa, false, false);
  const auto rb = __builtin_amdgcn_permlane32_swap(pb, pb, false, false);
  const unsigned pp[4] = {ra[0], rb[0], ra[1], rb[1]};
  asm volatile("s_waitcnt lgkmcnt(0)" : "+v"(vv[0][0]), "+v"(vv[0][1]), "+v"(vv[0][2]), "+v"(vv[0][3]), "+v"(vv[1][0]), "+v"(vv[1][1]),
      "+v"(vv[1][2]), "+v"(vv[1][3]), "+v"(vv[2][0]), "+v"(vv[2][1]), "+v"(vv[2][2]), "+v"(vv[2][3]), "+v"(vv[3][0]), "+v"(vv[3][1]),
      "+v"(vv[3][2]), "+v"(vv[3][3]), "+v"(vv[4][0]), "+v"(vv[4][1]), "+v"(vv[4][2]), "+v"(vv[4][3])::"memory");
#pragma unroll
  for (int e = 0; e < 5; ++e) {
    float o0 = 0.f, o1 = 0.f;
#pragma unroll
    for (int w2 = 0; w2 < 4; ++w2) {
      const unsigned x = vv[e][w2][0], y = vv[e][w2][1];
      o0 = dot2(__builtin_amdgcn_perm(y, x, 0x05040100u), pp[w2], o0);
      o1 = dot2(__builtin_amdgcn_perm(y, x, 0x07060302u), pp[w2], o1);
    }
    qp[h * 4 + e] = pk2(o0, o1);
  }
}
template <int MASK> DI void glds16_part(unsigned voff, const void* sbase, unsigned lds_dst) {
  unsigned keep;
  asm volatile("s_mov_b32 %0, m0\n\ts_mov_b32 m0, %3\n\ts_mov_b64 exec, %4\n\tglobal_load_lds_dwordx4 %1, %2\n\ts_mov_b64 exec, -1\n\ts_mov_b32 m0, %0"
               : "=&s"(keep) : "v"(voff), "s"(sbase), "s"(lds_dst), "i"(MASK) : "memory");
}
DI void glds16_sc1(unsigned voff, const void* sbase, unsigned lds_dst) {
  unsigned keep;
  asm volatile("s_mov_b32 %0, m0\n\ts_mov_b32 m0, %3\n\ts_nop 0\n\tglobal_load_lds_dwordx4 %1, %2 sc1\n\ts_mov_b32 m0, %0"
               : "=&s"(keep) : "v"(voff), "s"(sbase), "s"(lds_dst) : "memory");
}
DI void store_row(const char* stage, char* dst, int lane) {
  asm volatile("s_waitcnt lgkmcnt(0)" ::: "memory");
  const u32x4 a = *(const u32x4*)(stage + lane * 16);
  const u32x4 b = *(const u32x4*)(stage + 1024 + (lane & 7) * 16);
  asm volatile("global_store_dwordx4 %0, %1, off sc1\n\ts_nop 1" ::"v"(dst + lane * 16), "v"(a) : "memory");
  if (lane < 8) asm volatile("global_store_dwordx4 %0, %1, off offset:1024 sc1\n\ts_nop 1" ::"v"(dst + lane * 16), "v"(b) : "memory");
}
DI void signal_row(char* cnt, int lane) {
  asm volatile("s_waitcnt vmcnt(0)" ::: "memory");
  if (lane == 0) __hip_atomic_fetch_add((unsigned*)cnt, 1u, __ATOMIC_RELAXED, __HIP_MEMORY_SCOPE_AGENT);
}

__global__ __launch_bounds__(512) void k2_attn(char* __restrict__ ws, char* __restrict__ out) {
  extern __shared__ __attribute__((aligned(16))) char smem[];
  const int tid = threadIdx.x, lane = tid & 63;
  const int wave = __builtin_amdgcn_readfirstlane(tid >> 6);
  STAMP_DECL; STAMP(0);
  const int b = blockIdx.x;
  const int rblk = (b & 7) * 32 + (b >> 3);
  const int row = rblk * 8 + wave;
  const int c32 = lane & 31, h = lane >> 5;
  const unsigned lds0 = (unsigned)(uintptr_t)smem;
  const unsigned ctl = lds0 + L_CTL;
  const int ko = lane * 16;
  auto kv_fetch = [&](int i0, int n) {
#pragma unroll
    for (int i = i0; i < i0 + n; ++i) glds16(ko, ws + WS_KV + (wave + 8 * i) * 1024, lds0 + (wave + 8 * i) * 1024);
  };
  bf16x8 qf;
  unsigned qrem;
  {
    const char* qp = ws + WS_QB + (size_t)row * ROWB + c32 * 36;
    asm volatile("global_load_dwordx4 %0, %1, off" : "=v"(qf) : "v"(qp + h * 16) : "memory");
    asm volatile("global_load_dword %0, %1, off offset:32" : "=v"(qrem) : "v"(qp) : "memory");
    kv_fetch(0, 5);
  }
  if (tid < 16) *(unsigned*)(smem + L_CTL + tid * 4) = 0u;
  const int vrow = c32 < 19 ? c32 : (c32 == 28 ? 18 : 19);
  const int kro = h ? V_OFF + 18 * 16 : KR_OFF + c32 * 16;
  const int vo = V_OFF + h * 320 + vrow * 16;
  const int rg = rblk / 9, cg = rblk - rg * 9;
  const int nrt = rg < 16 ? 5 : 4, rt0 = rg < 16 ? rg * 5 : 16 + rg * 4;
  const bool has_tile = rblk < 252;
  const int fr = lane & 15, kg = lane >> 4;
  const unsigned xo = (lane >> 3) * ROWB + (((lane & 7) ^ (lane >> 3)) << 4);
  auto fetch_tile = [&](const char* src) {
    const char* s0 = src + (size_t)(rt0 * 16 + wave * 8) * ROWB;
    const char* s1 = src + (size_t)(rt0 * 16 + 64 + (wave & 1) * 8) * ROWB;
    if (nrt == 5) {
#pragma unroll
      for (int c = 0; c < 9; ++c) {
        glds16_sc1(xo, s0 + c * 128, lds0 + c * XC_B + wave * 1024);
        if (c % 3 == 2) {
          const int ce = c == 8 ? 8 : (c / 3) * 4 + (wave >> 1);
          glds16_sc1(xo, s1 + ce * 128, lds0 + ce * XC_B + (8 + (wave & 1)) * 1024);
        }
      }
    } else {
#pragma unroll
      for (int c = 0; c < 9; ++c) glds16_sc1(xo, s0 + c * 128, lds0 + c * XC_B + wave * 1024);
    }
  };
  const char* cnt_p = ws + WS_CNT + ((has_tile ? rt0 : 0) + (lane < nrt ? lane : 0)) * 128;
  const char* wfa = ws + WS_WF + (size_t)((has_tile ? cg * 4 : 0) + (wave & 3)) * 18432 + (wave >> 2) * 1024;
  const f32x16 z = {};
  f32x16 o = z;
  u32x4 wA[9], wB[9];
  STAMP(1);
  asm volatile("s_waitcnt vmcnt(0)" : "+v"(qf), "+v"(qrem)::"memory");
  bar_lgkm();
  STAMP(2);
  kv_fetch(5, 7);
  {
          const char* srcr[3];
#pragma unroll
          for (int i = 0; i < 3; ++i) {
            int r = wave + 8 * i;
            r = r > 21 ? 21 : r;
            const int gr = rblk * 8 + r - 14;
            srcr[i] = ws + WS_QKV + (size_t)(gr < 0 ? 0 : gr) * (3 * ROWB) + ROWB;
            glds16(ko, srcr[i], lds0 + LA_0 + r * 2304);
            glds16(ko, srcr[i] + 1024, lds0 + LA_0 + r * 2304 + 1024);
          }
          const char* src = ws + WS_QKV + (size_t)row * (3 * ROWB);
          glds16(ko, src, lds0 + LA_Q + wave * ROWB);
#pragma unroll
          for (int i = 0; i < 3; ++i) {
            int r = wave + 8 * i;
            r = r > 21 ? 21 : r;
            glds16_part<0xffff>(ko, srcr[i] + 2048, lds0 + LA_0 + r * 2304 + 2048);
          }
          glds16_part<0xff>(ko, src + 1024, lds0 + LA_Q + wave * ROWB + 1024);
  }
  const float ms = max_pass(smem + ko, smem + kro, qf, rem_word(qrem, 0.f, h), 1);
  const unsigned rw = rem_word(qrem, bf_lo(pk2(ms + M_OFF, 0.f)), h);
  {
    int kq = ko, krq = kro, vq = vo;
    const char *kp = smem + kq, *krp = smem + krq, *vp = smem + vq;
    const s16x4 qe = mk2(rw, 0u), qo = mk2(0u, rw);
    s16x4 k01 = *(const s16x4*)krp, k23 = *(const s16x4*)(krp + 8);
    f32x16 sa = MF16(*(const bf16x8*)kp, qf, MF8(k01, qe, z)), sb;
    bf16x8 kf = *(const bf16x8*)(kp + 1024), v0 = *(const bf16x8*)vp, v1 = v0;
    u32x4 p0 = {}, p1 = {};
    unsigned cnt_seen;
    STAMP(3); CSTAMP(10);
#define WF9(W, BASE) asm volatile( \
        "global_load_dwordx4 %0, %9, %10\n\tglobal_load_dwordx4 %1, %9, %10 offset:2048\n\t" \
        "global_load_dwordx4 %2, %11, %10 offset:-4096\n\tglobal_load_dwordx4 %3, %11, %10 offset:-2048\n\t" \
        "global_load_dwordx4 %4, %11, %10\n\tglobal_load_dwordx4 %5, %11, %10 offset:2048\n\t" \
        "global_load_dwordx4 %6, %12, %10 offset:-4096\n\tglobal_load_dwordx4 %7, %12, %10 offset:-2048\n\t" \
        "global_load_dwordx4 %8, %12, %10" \
        : "=&v"(W[0]), "=&v"(W[1]), "=&v"(W[2]), "=&v"(W[3]), "=&v"(W[4]), "=&v"(W[5]), "=&v"(W[6]), "=&v"(W[7]), "=&v"(W[8]) \
        : "v"(ko), "s"(BASE), "v"(ko + 8192), "v"(ko + 16384) : "memory")
    auto group = [&](auto gc) __attribute__((always_inline)) {
      constexpr int g = decltype(gc)::value;
      LSTAMP(2 * g);
      if constexpr (PRIO(g) != PRIO(g - 1)) __builtin_amdgcn_s_setprio(PRIO(g));
      if constexpr (g == 1) { wait_vm<11>(); lds_add(ctl + C_KVA); }
      if constexpr (g == 2) { wait_vm<0>(); lds_add(ctl + C_AROWS); }
      if constexpr (g == 3) lds_wait(ctl + C_KVA, 8);
      if constexpr (g == 3 || g == 4) {
        if ((wave >> 2) == 4 - g) {
          lds_wait(ctl + C_AROWS, 8);
          local_attn(smem, lds0, wave, c32, h, row);
          store_row(smem + LA_Q + wave * ROWB, ws + WS_OA + (size_t)row * ROWB, lane);
          lds_add(ctl + C_A2);
        }
      }
      if constexpr (g == 6) {
        signal_row(ws + WS_CNT + (row >> 4) * 128, lane);
        lds_wait(ctl + C_A2, 8);
        kv_fetch(12, 7);
      }
      if constexpr (g == 7) { wait_vm<0>(); lds_add(ctl + C_KVB); }
      if constexpr (g == 8) WF9(wA, wfa);
      if constexpr (g == 9) lds_wait(ctl + C_KVB, 8);
      if constexpr (g == 10) WF9(wB, wfa + 36 * 18432);
      if constexpr (g == 11) lds_add(ctl + C_PAST);
      if constexpr (g == 12) asm volatile("global_load_dword %0, %1, off sc1" : "=v"(cnt_seen) : "v"(cnt_p) : "memory");
      if constexpr (g == 13) {
        lds_wait(ctl + C_PAST, 8);
        asm volatile("s_waitcnt vmcnt(0)" : "+v"(cnt_seen)::"memory");
        if (has_tile) {
          if (!__all(cnt_seen >= 16u)) cnt_wait(cnt_p);
          fetch_tile(ws + WS_OA);
        }
      }
      if constexpr (g == 15) { wait_vm<0>(); lds_add(ctl + C_XA); }
      LSTAMP(2 * g + 1);
      SB;
      const int G = (g & 3) * GRP_B;
      phase<true>(sa, sb, k01, krp + G + GRP_B, qo, kf, kp + G + 2048, qf, v0, vp + G + VT_B, v1, vp + G + 640, p0, p1, o);
      phase<false>(sb, sa, k23, nullptr, qe, kf, kp + G + 3072, qf, v0, vp + G + 2 * VT_B, v1, vp + G + VT_B + 640, p0, p1, o);
      phase<true>(sa, sb, k23, krp + G + GRP_B + 8, qo, kf, kp + G + GRP_B, qf, v0, vp + G + 3 * VT_B, v1, vp + G + 2 * VT_B + 640, p0, p1, o);
      phase<false>(sb, sa, k01, nullptr, qe, kf, kp + G + GRP_B + 1024, qf, v0, vp + G + GRP_B, v1, vp + G + 3 * VT_B + 640, p0, p1, o);
      asm volatile("" : "+v"(o), "+v"(sa), "+v"(p1));
      if constexpr ((g & 3) == 3) {
        kq += 4 * GRP_B; krq += 4 * GRP_B; vq += 4 * GRP_B;
        asm volatile("" : "+v"(kq), "+v"(krq), "+v"(vq));
        kp = smem + kq; krp = smem + krq; vp = smem + vq;
      }
    };
#define GR(i) group(IC<i>{});
    GR(0) GR(1) GR(2) GR(3) GR(4) GR(5) GR(6) GR(7) GR(8) GR(9) GR(10) GR(11) GR(12) GR(13) GR(14) GR(15)
    o = MF16(v1, __builtin_bit_cast(bf16x8, p1), o);
  }
  STAMP(4); CSTAMP(11); LSTAMP(32);
  lds_add(ctl + C_DONE);
  float l = h ? o[12] : o[10];
  float big = l;
#pragma unroll
  for (int i = 0; i < 10; ++i) big = fmaxf(big, fabsf(o[i]));
  bool ok = (big < 1.0e37f) && (l > 0.f);
#pragma unroll
  for (int i = 0; i < 10; ++i) ok = ok && (o[i] == o[i]);
  if (!__all(ok)) {
    attn_exact(ws + WS_KV, ko, kro, vo, qf, qrem, h, o);
    l = h ? o[12] : o[10];
  }
  {
    const float inv = __builtin_amdgcn_rcpf(l);
    unsigned* dst = (unsigned*)(smem + L_OB + wave * ROWB + c32 * 36 + h * 8);
    dst[0] = pk2(o[0] * inv, o[1] * inv);
    dst[1] = pk2(o[2] * inv, o[3] * inv);
    dst[4] = pk2(o[4] * inv, o[5] * inv);
    dst[5] = pk2(o[6] * inv, o[7] * inv);
    if (!h) dst[8] = pk2(o[8] * inv, o[9] * inv);
    store_row(smem + L_OB + wave * ROWB, ws + WS_OB + (size_t)row * ROWB, lane);
  }
  char* cntB = ws + WS_CNT + (128 + (row >> 4)) * 128;
  if (!has_tile) { signal_row(cntB, lane); return; }
  const int xa = fr * 128 + ((((wave >> 2) * 4 + kg) ^ (fr & 7)) << 4);
  char* pw = smem + L_P + (wave >> 2) * P_HALF + fr * P_LD + (wave & 3) * 64 + kg * 16;
  f32x4 acc[5];
  auto mm3 = [&](u32x4 (&w)[9], int c0) {
#pragma unroll
    for (int c = c0; c < c0 + 3; ++c) {
      bf16x8 xf[5];
#pragma unroll
      for (int m = 0; m < 5; ++m) xf[m] = *(const bf16x8*)(smem + c * XC_B + xa + m * 2048);
#pragma unroll
      for (int m = 0; m < 5; ++m) acc[m] = MFMA16(__builtin_bit_cast(bf16x8, w[c]), xf[m], c ? acc[m] : f32x4{});
    }
  };
  auto put = [&] {
#pragma unroll
    for (int m = 0; m < 5; ++m) *(f32x4*)(pw + m * 16 * P_LD) = acc[m];
  };
  auto tile_out = [&](int br) {
#pragma unroll
    for (int i = 0; i < 2; ++i) {
      const int r = (tid >> 3) + 64 * i, c = tid & 7;
      if (r < nrt * 16) {
        const char* p = smem + L_P + r * P_LD + c * 32;
        const f32x4 a0 = *(const f32x4*)p, a1 = *(const f32x4*)(p + 16);
        const f32x4 b0 = *(const f32x4*)(p + P_HALF), b1 = *(const f32x4*)(p + P_HALF + 16);
        const u32x4 u = {pk2(a0[0] + b0[0], a0[1] + b0[1]), pk2(a0[2] + b0[2], a0[3] + b0[3]),
                         pk2(a1[0] + b1[0], a1[1] + b1[1]), pk2(a1[2] + b1[2], a1[3] + b1[3])};
        ST16(out + (size_t)(rt0 * 16 + r) * (2 * ROWB) + br * ROWB + cg * 128 + c * 16, u);
      }
    }
  };
  signal_row(cntB, lane);
  STAMP(5);
  lds_wait(ctl + C_XA, 8);
  mm3(wA, 0);
  mm3(wA, 3);
  mm3(wA, 6);
  lds_wait(ctl + C_DONE, 8);
  put();
  if (wave == 0) cnt_wait(cnt_p + 128 * 128);
  bar_lgkm();
  STAMP(6);
  fetch_tile(ws + WS_OB);
  tile_out(0);
  if (nrt == 5) wait_vm<8>(); else wait_vm<6>();
  bar_lgkm();
  STAMP(7);
  mm3(wB, 0);
  if (nrt == 5) wait_vm<4>(); else wait_vm<3>();
  bar_lgkm();
  mm3(wB, 3);
  wait_vm<0>();
  bar_lgkm();
  mm3(wB, 6);
  put();
  bar_lgkm();
  STAMP(8);
  tile_out(1);
  STAMP(9);
  STAMP_OUT(1, ws); LSTAMP_OUT(ws);
}

extern "C" void kernel_launch(void* const* d_in, const int* in_sizes, int n_in, void* d_out, int out_size,
                              void* d_ws, size_t ws_size, hipStream_t stream) {
  (void)in_sizes; (void)n_in; (void)out_size; (void)ws_size;
  (void)hipFuncSetAttribute((const void*)k1_proj, hipFuncAttributeMaxDynamicSharedMemorySize, K1_LDS);
  (void)hipFuncSetAttribute((const void*)k2_attn, hipFuncAttributeMaxDynamicSharedMemorySize, K2_LDS_ALL);
  char* ws = (char*)d_ws;
  k1_proj<<<256, 512, K1_LDS, stream>>>((const char*)d_in[0], (const char*)d_in[1], (const char*)d_in[3], (const char*)d_in[4],
                                        (const char*)d_in[5], (const char*)d_in[6], (const char*)d_in[2], (const char*)d_in[7], ws);
  k2_attn<<<256, 512, K2_LDS_ALL, stream>>>(ws, (char*)d_out);
}
```

```cpp
#include <hip/hip_runtime.h>
#include <hip/hip_bf16.h>
#include <stdint.h>

typedef __bf16 bf16x2 __attribute__((ext_vector_type(2)));
typedef __bf16 bf16x8 __attribute__((ext_vector_type(8)));
typedef short s16x4 __attribute__((ext_vector_type(4)));
typedef float f32x2 __attribute__((ext_vector_type(2)));
typedef float f32x4 __attribute__((ext_vector_type(4)));
typedef float f32x16 __attribute__((ext_vector_type(16)));
typedef unsigned u32x2 __attribute__((ext_vector_type(2)));
typedef unsigned u32x4 __attribute__((ext_vector_type(4)));
#define LDSP(T) __attribute__((address_space(3))) T*
#define DI __device__ __forceinline__
#define STAMP_DECL
#define STAMP(i) do {} while (0)
#define CSTAMP(i) do {} while (0)
#define LSTAMP(i) do {} while (0)
#define LSTAMP_OUT(w) do {} while (0)
#define STAMP_ARG
#define STAMP_PASS
#define STAMP_OUT(kid, wsp) do {} while (0)

#define K1UNROLL _Pragma("unroll")
#define PRIO(g) ((g) < 0 ? -1 : 3 - (((g) >> 1) & 3))
constexpr int SEQ = 2048, DM = 576;
constexpr int ROWB = DM * 2;
constexpr float LOG2E = 1.4426950408889634f;
constexpr float QSCALE = 0.23570226039551584f * LOG2E;

constexpr int GRP_B = 9728, KR_OFF = 4096, V_OFF = 4608, VT_B = 1280;
constexpr int KV_BYTES = 16 * GRP_B;
constexpr size_t WS_KV = 0;
constexpr size_t WS_QB = WS_KV + KV_BYTES;
constexpr size_t WS_QKV = WS_QB + (size_t)SEQ * ROWB;
constexpr size_t WS_OA = WS_QKV + (size_t)SEQ * 3 * ROWB;
constexpr size_t WS_OB = WS_OA + (size_t)SEQ * ROWB;
constexpr size_t WS_WF = WS_OB + (size_t)SEQ * ROWB;
constexpr size_t WS_CNT = WS_WF + (size_t)72 * 18432;

template <int N> struct IC { static constexpr int value = N; };
DI unsigned pk2(float a, float b) {
  f32x2 f = {a, b};
  return __builtin_bit_cast(unsigned, __builtin_convertvector(f, bf16x2));
}
DI float dot2(unsigned a, unsigned b, float c) {
  return __builtin_amdgcn_fdot2_f32_bf16(__builtin_bit_cast(bf16x2, a), __builtin_bit_cast(bf16x2, b), c, false);
}
DI float bf_lo(unsigned u) { return __uint_as_float(u << 16); }
DI float bf_hi(unsigned u) { return __uint_as_float(u & 0xffff0000u); }

DI void glds16(unsigned voff, const void* sbase, unsigned lds_dst) {
  unsigned keep;
  asm volatile("s_mov_b32 %0, m0\n\ts_mov_b32 m0, %3\n\ts_nop 0\n\tglobal_load_lds_dwordx4 %1, %2\n\ts_mov_b32 m0, %0"
               : "=&s"(keep) : "v"(voff), "s"(sbase), "s"(lds_dst) : "memory");
}
DI void glds4(unsigned voff, const void* sbase, unsigned lds_dst) {
  unsigned keep;
  asm volatile("s_mov_b32 %0, m0\n\ts_mov_b32 m0, %3\n\ts_nop 0\n\tglobal_load_lds_dword %1, %2\n\ts_mov_b32 m0, %0"
               : "=&s"(keep) : "v"(voff), "s"(sbase), "s"(lds_dst) : "memory");
}
#define CODE_PREFETCH(SYM, voff, lds_dst) do { unsigned keep_; unsigned long long pc_; \
    asm volatile("s_getpc_b64 s[98:99]\n\ts_add_u32 s98, s98, " SYM "@rel32@lo+4\n\ts_addc_u32 s99, s99, " SYM "@rel32@hi+12\n\t" \
                 "s_mov_b32 %0, m0\n\ts_mov_b32 m0, %2\n\ts_nop 0\n\tglobal_load_lds_dword %1, s[98:99]\n\ts_mov_b32 m0, %0" \
                 : "=&s"(keep_) : "v"(voff), "s"(lds_dst) : "memory", "scc", "s98", "s99"); (void)pc_; } while (0)
#define K1_SYM "_Z7k1_projPKcS0_S0_S0_S0_S0_S0_S0_Pc"
#define K2_SYM "_Z7k2_attnPcS_"
constexpr int K1_CODE = 15616, K2_CODE = 32768;
template <int N> DI void wait_vm() { asm volatile("s_waitcnt vmcnt(%0)" ::"i"(N) : "memory"); }
DI void bar_lgkm() { asm volatile("s_waitcnt lgkmcnt(0)\n\ts_barrier" ::: "memory"); }
DI bf16x8 tr_frag(const char* p) {
  s16x4 lo = __builtin_amdgcn_ds_read_tr16_b64_v4i16((LDSP(s16x4))(p));
  s16x4 hi = __builtin_amdgcn_ds_read_tr16_b64_v4i16((LDSP(s16x4))(p + 256));
  return __builtin_bit_cast(bf16x8, __builtin_shufflevector(lo, hi, 0, 1, 2, 3, 4, 5, 6, 7));
}
#define ST16(p, v) __builtin_nontemporal_store(v, (u32x4*)(p))
#define MFMA16(a, b, c) __builtin_amdgcn_mfma_f32_16x16x32_bf16(a, b, c, 0, 0, 0)

constexpr int K1_XB = 20480, K1_STAGE = 36864, K1_LDS = 4 * K1_STAGE + 256;
constexpr int K1_RB = 13, K1_ACT = 14, K1_BCT = 5, K1_GEMM = K1_RB * (K1_ACT + K1_BCT);

template <int MODE>
DI void k1_loader(const char* X, int row0, const char* W0, const char* W1, int ldw, int colB0, int colB1,
                  const char* wk, const char* wv, int rot, unsigned lds0, int l, int lane) {
  unsigned xo[5], wo[4], eo[8];
  if (MODE != 2) {
#pragma unroll
    for (int i = 0; i < 5; ++i) {
      int r = row0 + (l + 4 * i) * 8 + (lane >> 3);
      r = r > SEQ - 1 ? SEQ - 1 : r;
      xo[i] = r * ROWB + (((lane & 7) ^ (lane >> 3)) << 4);
    }
  }
#pragma unroll
  for (int i = 0; i < 4; ++i) {
    const int kp = l + 4 * (i >> 1);
    const int k = kp * 8 + ((lane >> 2) & 7);
    wo[i] = k * ldw + ((i & 1) ? colB1 : colB0) + (lane >> 5) * 64 + (((lane & 3) ^ ((kp & 1) << 1)) << 4);
  }
  if (MODE == 1) {
#pragma unroll
    for (int i = 0; i < 4; ++i) {
      const int kp = l + 4 * (i >> 1);
      const int k = kp * 8 + (i & 1) * 4 + (lane >> 4);
      int dw = (lane & 15) ^ ((kp & 1) << 3);
      dw = dw > 8 ? 8 : dw;
      eo[i] = k * 36 + dw * 4;
    }
  }
  auto issue = [&](int j) {
    int ks = j + rot; ks = ks >= 9 ? ks - 9 : ks;
    const unsigned sb = lds0 + (j & 3) * K1_STAGE;
    if (MODE != 2) {
      const char* xb = X + ks * 128;
#pragma unroll
      for (int i = 0; i < 5; ++i) glds16(xo[i], xb, sb + (l + 4 * i) * 1024);
    }
    const char* w0 = W0 + (size_t)ks * 64 * ldw;
    const char* w1 = W1 + (size_t)ks * 64 * ldw;
#pragma unroll
    for (int i = 0; i < 4; ++i) {
      if (MODE == 1 && (i & 1)) continue;
      glds16(wo[i], (i & 1) ? w1 : w0, sb + K1_XB + (l + 4 * (i >> 1)) * 2048 + (i & 1) * 1024);
    }
    if (MODE == 1) {
      const char* kb = wk + ks * (64 * 36);
      const char* vb = wv + ks * (64 * 36);
#pragma unroll
      for (int i = 0; i < 4; ++i) {
        const unsigned d = sb + K1_XB + (l + 4 * (i >> 1)) * 2048 + 1024 + (i & 1) * 256;
        glds4(eo[i], kb, d);
        glds4(eo[i], vb, d + 512);
      }
    }
  };
  constexpr int P = MODE == 0 ? 9 : (MODE == 1 ? 15 : 4);
#pragma unroll 1
  for (int t = 0; t < 9; ++t) {
    issue(t);
    if (t == 1) { wait_vm<P>(); bar_lgkm(); }
    else if (t >= 3) { wait_vm<2 * P>(); bar_lgkm(); }
  }
  wait_vm<P>(); bar_lgkm();
  wait_vm<0>(); bar_lgkm();
  bar_lgkm();
}

__global__ __launch_bounds__(512) void k1_proj(const char* __restrict__ xA, const char* __restrict__ wqkv,
                                               const char* __restrict__ xB, const char* __restrict__ wq,
                                               const char* __restrict__ wk, const char* __restrict__ wv,
                                               const char* __restrict__ woA, const char* __restrict__ woB,
                                               char* __restrict__ ws) {
  extern __shared__ __attribute__((aligned(16))) char smem[];
  const int tid = threadIdx.x, lane = tid & 63;
  const int wave = __builtin_amdgcn_readfirstlane(tid >> 6);
  STAMP_DECL; STAMP(0);
  const unsigned lds0 = (unsigned)(uintptr_t)smem;
  if (wave < 4) {
    const int o1 = tid * 128 < K1_CODE ? tid * 128 : 0, o2 = tid * 128 < K2_CODE ? tid * 128 : 0;
    CODE_PREFETCH(K1_SYM, o1, lds0 + 4 * K1_STAGE);
    CODE_PREFETCH(K2_SYM, o2, lds0 + 4 * K1_STAGE);
  }
  const int b = blockIdx.x;
  const int unit = (b & 7) * 32 + (b >> 3);
  const int fr = lane & 15, kg = lane >> 4;
  const int rot = unit % 9;
  const int tro = kg * 2048 + (fr >> 2) * 64 + (fr & 3) * 8;
  const int trE = K1_XB + tro + ((kg & 1) << 5), trO = K1_XB + tro + (((kg & 1) ^ 1) << 5);

  if (unit >= K1_GEMM) {
    const int s = unit - K1_GEMM;
    if (s == 0 && tid < 256) *(unsigned*)(ws + WS_CNT + tid * 128) = 0u;
    if (wave >= 4) {
      const int c0 = s * 128, c1 = c0 + 64;
      k1_loader<2>(nullptr, 0, c0 < DM ? woA : woB, c1 < DM ? woA : woB, ROWB, (c0 < DM ? c0 : c0 - DM) * 2, (c1 < DM ? c1 : c1 - DM) * 2,
                   nullptr, nullptr, rot, lds0, wave - 4, lane);
    } else {
      bar_lgkm();
      char* dst = ws + WS_WF + (size_t)(s * 8 + wave * 2) * 18432 + lane * 16;
#pragma unroll 1
      for (int j = 0; j < 9; ++j) {
        int ks = j + rot; ks = ks >= 9 ? ks - 9 : ks;
        const char* sb = smem + (j & 3) * K1_STAGE + wave * 512;
        const bf16x8 f0 = tr_frag(sb + trE), f1 = tr_frag(sb + trO), f2 = tr_frag(sb + trE + 8192), f3 = tr_frag(sb + trO + 8192);
        bar_lgkm();
        ST16(dst + ks * 2048, __builtin_bit_cast(u32x4, f0));
        ST16(dst + ks * 2048 + 18432, __builtin_bit_cast(u32x4, f1));
        ST16(dst + ks * 2048 + 1024, __builtin_bit_cast(u32x4, f2));
        ST16(dst + ks * 2048 + 18432 + 1024, __builtin_bit_cast(u32x4, f3));
      }
    }
    STAMP(9);
    STAMP_OUT(0, ws);
    return;
  }
  const bool isA = unit < K1_RB * K1_ACT;
  int rb, ct;
  if (isA) {
    if (unit < 70) { ct = unit / 5; rb = unit - ct * 5; }
    else { const int u = unit - 70, band = u >= 56 ? 1 : 0, v = u - band * 56; ct = v >> 2; rb = 5 + band * 4 + (v & 3); }
  } else { const int u = unit - K1_RB * K1_ACT; rb = u / K1_BCT; ct = u - rb * K1_BCT; }
  const int row0 = rb * 160;
  const bool last = ct == (isA ? K1_ACT - 1 : K1_BCT - 1);
  const int wm = wave & 1, wn = (wave >> 1) & 1;
  constexpr int OT_LD = 272;
  if (wave >= 4) {
    if (isA) k1_loader<0>(xA, row0, wqkv, wqkv, 3 * ROWB, ct * 256, ct * 256 + (last ? 0 : 128), nullptr, nullptr, rot, lds0, wave - 4, lane);
    else if (!last) k1_loader<0>(xB, row0, wq, wq, ROWB, ct * 256, ct * 256 + 128, nullptr, nullptr, rot, lds0, wave - 4, lane);
    else k1_loader<1>(xB, row0, wq, wq, ROWB, ct * 256, 0, wk, wv, rot, lds0, wave - 4, lane);
  } else {
    f32x4 acc[5][4] = {};
    const int xo0 = (wm * 80 + fr) * 128 + ((kg ^ (fr & 7)) << 4), xo1 = xo0 ^ 64;
    const int wE = trE + wn * 1024, wO = trO + wn * 1024;
    bf16x8 xa[5], wa[4], xb[5], wb[4];
    auto rd = [&](const char* sb, int kk, bf16x8 (&xf)[5], bf16x8 (&wf)[4]) {
#pragma unroll
      for (int m = 0; m < 5; ++m) xf[m] = *(const bf16x8*)(sb + (kk ? xo1 : xo0) + m * 2048);
#pragma unroll
      for (int n = 0; n < 4; ++n) wf[n] = tr_frag(sb + ((n & 1) ? wO : wE) + kk * 8192 + (n >> 1) * 512);
    };
    auto mm = [&](bf16x8 (&xf)[5], bf16x8 (&wf)[4]) {
#pragma unroll
      for (int n = 0; n < 4; ++n)
#pragma unroll
        for (int m = 0; m < 5; ++m) acc[m][n] = MFMA16(wf[n], xf[m], acc[m][n]);
    };
    bar_lgkm();
    STAMP(1); CSTAMP(10);
    rd(smem, 0, xa, wa);
#define ILV() do { _Pragma("unroll") for (int q_ = 0; q_ < 13; ++q_) { __builtin_amdgcn_sched_group_barrier(0x8, 1, 0); __builtin_amdgcn_sched_group_barrier(0x100, 1, 0); } \
                   __builtin_amdgcn_sched_group_barrier(0x8, 7, 0); } while (0)
    K1UNROLL
    for (int j = 0; j < 9; ++j) {
      rd(smem + (j & 3) * K1_STAGE, 1, xb, wb);
      mm(xa, wa);
      ILV();
      __builtin_amdgcn_sched_barrier(0);
      bar_lgkm();
      rd(smem + ((j + 1) & 3) * K1_STAGE, 0, xa, wa);
      mm(xb, wb);
      ILV();
      __builtin_amdgcn_sched_barrier(0);
    }
    STAMP(2); CSTAMP(11);
    const bool scale = !isA && !(last && wn == 1);
#pragma unroll
    for (int m = 0; m < 5; ++m)
#pragma unroll
      for (int n = 0; n < 4; ++n) {
        u32x2 u = {pk2(acc[m][n][0], acc[m][n][1]), pk2(acc[m][n][2], acc[m][n][3])};
        if (scale) {
          u[0] = pk2(bf_lo(u[0]) * QSCALE, bf_hi(u[0]) * QSCALE);
          u[1] = pk2(bf_lo(u[1]) * QSCALE, bf_hi(u[1]) * QSCALE);
        }
        *(u32x2*)(smem + (wm * 80 + m * 16 + fr) * OT_LD + wn * 128 + n * 32 + kg * 8) = u;
      }
  }
  bar_lgkm();
  STAMP(3);
  {
    char* dst = isA ? ws + WS_QKV : ws + WS_QB;
    const int ld = isA ? 3 * ROWB : ROWB;
    const int c = tid & 15;
    if (!(last && c >= 8)) {
#pragma unroll
      for (int i = 0; i < 5; ++i) {
        const int r = (tid >> 4) + 32 * i;
        if (row0 + r < SEQ) ST16(dst + (size_t)(row0 + r) * ld + ct * 256 + c * 16, *(const u32x4*)(smem + r * OT_LD + c * 16));
      }
    }
  }
  if (!isA && last) {
    char* kv = ws + WS_KV;
    if (tid < 320) {
      const int r = tid >> 1, hh = tid & 1, key = row0 + r;
      if (key < SEQ) {
        const int t = key >> 5, k32 = key & 31;
        char* g = kv + (t >> 2) * GRP_B;
        *(u32x4*)(g + (t & 3) * 1024 + (hh * 32 + k32) * 16) = *(const u32x4*)(smem + r * OT_LD + 128 + hh * 16);
        if (hh == 0) *(unsigned*)(g + KR_OFF + k32 * 16 + (t & 3) * 4) = *(const unsigned*)(smem + r * OT_LD + 160);
      }
    }
    if (tid < 400) {
      const int blk = tid / 20, d = tid - blk * 20;
      const int tl = blk >> 2, s = (blk >> 1) & 1, hh = blk & 1;
      const int t = (row0 >> 5) + tl;
      if (t < 64) {
        u32x4 o;
        if (d < 18) {
          const char* src = smem + (tl * 32 + s * 16 + hh * 4) * OT_LD + 192 + d * 2;
#pragma unroll
          for (int e = 0; e < 4; ++e) {
            const int r = (e >> 1) * 8 + (e & 1) * 2;
            o[e] = (unsigned)*(const unsigned short*)(src + r * OT_LD) | ((unsigned)*(const unsigned short*)(src + (r + 1) * OT_LD) << 16);
          }
        } else {
          const unsigned f = d == 18 ? 0x3f803f80u : 0u;
          o = u32x4{f, f, f, f};
        }
        *(u32x4*)(kv + (t >> 2) * GRP_B + V_OFF + (t & 3) * VT_B + (s * 2 + hh) * 320 + d * 16) = o;
      }
    }
  }
  STAMP(9);
  STAMP_OUT(0, ws);
}

constexpr int K2_LDS = KV_BYTES + 6144;
constexpr float M_OFF = 96.0f;
#define SB __builtin_amdgcn_sched_barrier(0)
#define MF16(a, b, c) __builtin_amdgcn_mfma_f32_32x32x16_bf16(a, b, c, 0, 0, 0)
#define MF8(a, b, c) __builtin_amdgcn_mfma_f32_32x32x8bf16_1k(a, b, c, 0, 0, 0)
#define EX(i) cur[i] = __builtin_amdgcn_exp2f(cur[i])

DI float xhalf(float x);
DI float max16(const f32x16& s, float m) {
#pragma unroll
  for (int i = 0; i < 16; i += 2) m = __builtin_fmaxf(__builtin_fmaxf(s[i], s[i + 1]), m);
  return m;
}
#define E(i) EX(i); SB;
#define C0(i) p0[i] = pk2(cur[2 * i], cur[2 * i + 1]); SB;
#define C1(i) p1[i] = pk2(cur[8 + 2 * i], cur[9 + 2 * i]); SB;
template <bool RELOAD>
DI void phase(f32x16& cur, f32x16& nxt, s16x4& kra, const char* krp, const s16x4 qr, bf16x8& kf, const char* kp2, const bf16x8 qf,
              bf16x8& v0, const char* v0p, bf16x8& v1, const char* v1p, u32x4& p0, u32x4& p1, f32x16& o) {
  const f32x16 z = {};
  nxt = MF8(kra, qr, z); SB;
  if (RELOAD) { kra = *(const s16x4*)krp; SB; }
  E(0) E(1) E(2) E(3) C0(0) E(4)
  o = MF16(v1, __builtin_bit_cast(bf16x8, p1), o); SB;
  v1 = *(const bf16x8*)v1p; SB;
  C0(1) E(5) E(6) E(7) C0(2) E(8)
  nxt = MF16(kf, qf, nxt); SB;
  kf = *(const bf16x8*)kp2; SB;
  C0(3) E(9) E(10) E(11) C1(0) E(12)
  o = MF16(v0, __builtin_bit_cast(bf16x8, p0), o); SB;
  v0 = *(const bf16x8*)v0p; SB;
  C1(1) E(13) E(14) E(15) C1(2) C1(3)
}
DI unsigned rem_word(unsigned qrem, float m, int h) {
  const unsigned hi = pk2(-m, 0.f) & 0xffffu;
  const unsigned lo = pk2(-m - bf_lo(hi), 0.f) & 0xffffu;
  return h ? (hi | (lo << 16)) : qrem;
}
DI s16x4 mk2(unsigned a, unsigned b) { u32x2 r = {a, b}; return __builtin_bit_cast(s16x4, r); }

DI float max_pass(const char* kp, const char* krp, const bf16x8 qf, unsigned rw, int npair) {
  const s16x4 qe = mk2(rw, 0u), qo = mk2(0u, rw);
  const f32x16 z = {};
  float m = -3.0e38f;
  for (int i = 0; i < npair; ++i) {
    const int G = (i >> 1) * GRP_B, hf = i & 1;
    const s16x4 kr = *(const s16x4*)(krp + G + hf * 8);
    m = max16(MF16(*(const bf16x8*)(kp + G + hf * 2048), qf, MF8(kr, qe, z)), m);
    m = max16(MF16(*(const bf16x8*)(kp + G + hf * 2048 + 1024), qf, MF8(kr, qo, z)), m);
  }
  return fmaxf(m, xhalf(m));
}
DI void attn_exact(const char* img, int ko, int kro, int vo, const bf16x8 qf, unsigned qrem, int h, f32x16& o) {
  const float mt = max_pass(img + ko, img + kro, qf, rem_word(qrem, 0.f, h), 32);
  const unsigned rw = rem_word(qrem, mt, h);
  const s16x4 qe = mk2(rw, 0u), qo = mk2(0u, rw);
  const f32x16 z = {};
  o = z;
  for (int t = 0; t < 64; ++t) {
    const char* g = img + (t >> 2) * GRP_B;
    const s16x4 kr = *(const s16x4*)(g + kro + (t & 2) * 4);
    f32x16 cur = MF16(*(const bf16x8*)(g + ko + (t & 3) * 1024), qf, MF8(kr, (t & 1) ? qo : qe, z));
    u32x4 p0, p1;
#pragma unroll
    for (int i = 0; i < 16; ++i) EX(i);
#pragma unroll
    for (int i = 0; i < 4; ++i) { p0[i] = pk2(cur[2 * i], cur[2 * i + 1]); p1[i] = pk2(cur[8 + 2 * i], cur[9 + 2 * i]); }
    o = MF16(*(const bf16x8*)(g + vo + (t & 3) * VT_B), __builtin_bit_cast(bf16x8, p0), o);
    o = MF16(*(const bf16x8*)(g + vo + (t & 3) * VT_B + 640), __builtin_bit_cast(bf16x8, p1), o);
  }
}

constexpr int LA_0 = 98304;
constexpr int LA_Q = LA_0 + 22 * 2304;
constexpr int L_OB = 94208;
constexpr int L_P = 104448;
constexpr int P_LD = 272, P_HALF = 80 * P_LD;
constexpr int XC_B = 10240;
constexpr int L_CTL = K2_LDS;
constexpr int K2_LDS_ALL = K2_LDS + 64 + 256;
enum { C_KVA = 0, C_KVB = 4, C_PAST = 8, C_AROWS = 12, C_A2 = 16, C_XA = 20, C_DONE = 24, C_PROG = 32 };

DI void lds_add(unsigned addr) {
  asm volatile("s_waitcnt lgkmcnt(0)\n\ts_mov_b64 exec, 1\n\tds_add_u32 %0, %1\n\ts_mov_b64 exec, -1" ::"v"(addr), "v"(1u) : "memory");
}
DI void lds_wait(unsigned addr, unsigned n) {
  for (;;) {
    unsigned r;
    asm volatile("ds_read_b32 %0, %1\n\ts_waitcnt lgkmcnt(0)" : "=&v"(r) : "v"(addr) : "memory");
    if (__builtin_amdgcn_readfirstlane(r) >= n) break;
    __builtin_amdgcn_s_sleep(2);
  }
}
DI void cnt_wait(const char* p) {
  for (int spin = 0; spin < (1 << 22); ++spin) {
    unsigned r;
    asm volatile("global_load_dword %0, %1, off sc1\n\ts_waitcnt vmcnt(0)" : "=&v"(r) : "v"(p) : "memory");
    if (__all(r >= 16u)) break;
    __builtin_amdgcn_s_sleep(2);
  }
}
DI float xhalf(float x) {
  const unsigned u = __float_as_uint(x);
  const auto r = __builtin_amdgcn_permlane32_swap(u, u, false, false);
  return __uint_as_float((threadIdx.x & 32) ? r[0] : r[1]);
}
DI void local_attn(char* smem, unsigned lds0, int wave, int c32, int h, int row) {
  unsigned* qp = (unsigned*)(smem + LA_Q + wave * ROWB + c32 * 36);
  unsigned q[9];
#pragma unroll
  for (int e = 0; e < 9; ++e) q[e] = qp[e];
  const unsigned kb = lds0 + LA_0 + (wave + 8 - h * 8) * 2304 + c32 * 36;
  const unsigned vb = lds0 + LA_0 + wave * 2304 + ROWB + c32 * 36 + h * 16;
  u32x2 kk[9][2], vv[5][4];
#pragma unroll
  for (int e = 0; e < 9; ++e)
    asm volatile("ds_read2st64_b32 %0, %2 offset0:54 offset1:36\n\tds_read2st64_b32 %1, %2 offset0:18 offset1:0"
                 : "=&v"(kk[e][0]), "=&v"(kk[e][1]) : "v"(kb + e * 4) : "memory");
  asm volatile("s_waitcnt lgkmcnt(0)" : "+v"(kk[0][0]), "+v"(kk[0][1]), "+v"(kk[1][0]), "+v"(kk[1][1]), "+v"(kk[2][0]), "+v"(kk[2][1]),
      "+v"(kk[3][0]), "+v"(kk[3][1]), "+v"(kk[4][0]), "+v"(kk[4][1]), "+v"(kk[5][0]), "+v"(kk[5][1]), "+v"(kk[6][0]), "+v"(kk[6][1]),
      "+v"(kk[7][0]), "+v"(kk[7][1]), "+v"(kk[8][0]), "+v"(kk[8][1])::"memory");
#pragma unroll
  for (int e = 0; e < 5; ++e)
    asm volatile("ds_read2st64_b32 %0, %4 offset0:126 offset1:108\n\tds_read2st64_b32 %1, %4 offset0:90 offset1:72\n\t"
                 "ds_read2st64_b32 %2, %4 offset0:54 offset1:36\n\tds_read2st64_b32 %3, %4 offset0:18 offset1:0"
                 : "=&v"(vv[e][0]), "=&v"(vv[e][1]), "=&v"(vv[e][2]), "=&v"(vv[e][3]) : "v"(vb + e * 4) : "memory");
  float lg[4] = {0.f, 0.f, 0.f, 0.f};
#pragma unroll
  for (int e = 0; e < 9; ++e) {
    const unsigned k0 = kk[e][0][0], k1 = kk[e][0][1], k2 = kk[e][1][0], k3 = kk[e][1][1];
    lg[0] = dot2(q[e], k0, lg[0]); lg[1] = dot2(q[e], k1, lg[1]); lg[2] = dot2(q[e], k2, lg[2]); lg[3] = dot2(q[e], k3, lg[3]);
  }
#pragma unroll
  for (int w = 0; w < 4; ++w) lg[w] = row - 2 * (h * 4 + w) >= 0 ? lg[w] * QSCALE : -1.0e9f * LOG2E;
  float mx = fmaxf(fmaxf(lg[0], lg[1]), fmaxf(lg[2], lg[3]));
  mx = fmaxf(mx, xhalf(mx));
  float sum = 0.f;
#pragma unroll
  for (int w = 0; w < 4; ++w) { lg[w] = __builtin_amdgcn_exp2f(lg[w] - mx); sum += lg[w]; }
  sum += xhalf(sum);
  const float inv = __builtin_amdgcn_rcpf(sum);
  const unsigned pa = pk2(lg[0] * inv, lg[1] * inv), pb = pk2(lg[2] * inv, lg[3] * inv);
  const auto ra = __builtin_amdgcn_permlane32_swap(pa, pa, false, false);
  const auto rb = __builtin_amdgcn_permlane32_swap(pb, pb, false, false);
  const unsigned pp[4] = {ra[0], rb[0], ra[1], rb[1]};
  asm volatile("s_waitcnt lgkmcnt(0)" : "+v"(vv[0][0]), "+v"(vv[0][1]), "+v"(vv[0][2]), "+v"(vv[0][3]), "+v"(vv[1][0]), "+v"(vv[1][1]),
      "+v"(vv[1][2]), "+v"(vv[1][3]), "+v"(vv[2][0]), "+v"(vv[2][1]), "+v"(vv[2][2]), "+v"(vv[2][3]), "+v"(vv[3][0]), "+v"(vv[3][1]),
      "+v"(vv[3][2]), "+v"(vv[3][3]), "+v"(vv[4][0]), "+v"(vv[4][1]), "+v"(vv[4][2]), "+v"(vv[4][3])::"memory");
#pragma unroll
  for (int e = 0; e < 5; ++e) {
    float o0 = 0.f, o1 = 0.f;
#pragma unroll
    for (int w2 = 0; w2 < 4; ++w2) {
      const unsigned x = vv[e][w2][0], y = vv[e][w2][1];
      o0 = dot2(__builtin_amdgcn_perm(y, x, 0x05040100u), pp[w2], o0);
      o1 = dot2(__builtin_amdgcn_perm(y, x, 0x07060302u), pp[w2], o1);
    }
    qp[h * 4 + e] = pk2(o0, o1);
  }
}
template <int MASK> DI void glds16_part(unsigned voff, const void* sbase, unsigned lds_dst) {
  unsigned keep;
  asm volatile("s_mov_b32 %0, m0\n\ts_mov_b32 m0, %3\n\ts_mov_b64 exec, %4\n\tglobal_load_lds_dwordx4 %1, %2\n\ts_mov_b64 exec, -1\n\ts_mov_b32 m0, %0"
               : "=&s"(keep) : "v"(voff), "s"(sbase), "s"(lds_dst), "i"(MASK) : "memory");
}
DI void glds16_sc1(unsigned voff, const void* sbase, unsigned lds_dst) {
  unsigned keep;
  asm volatile("s_mov_b32 %0, m0\n\ts_mov_b32 m0, %3\n\ts_nop 0\n\tglobal_load_lds_dwordx4 %1, %2 sc1\n\ts_mov_b32 m0, %0"
               : "=&s"(keep) : "v"(voff), "s"(sbase), "s"(lds_dst) : "memory");
}
DI void store_row(const char* stage, char* dst, int lane) {
  asm volatile("s_waitcnt lgkmcnt(0)" ::: "memory");
  const u32x4 a = *(const u32x4*)(stage + lane * 16);
  const u32x4 b = *(const u32x4*)(stage + 1024 + (lane & 7) * 16);
  asm volatile("global_store_dwordx4 %0, %1, off sc1\n\ts_nop 1" ::"v"(dst + lane * 16), "v"(a) : "memory");
  if (lane < 8) asm volatile("global_store_dwordx4 %0, %1, off offset:1024 sc1\n\ts_nop 1" ::"v"(dst + lane * 16), "v"(b) : "memory");
}
DI void signal_row(char* cnt, int lane) {
  asm volatile("s_waitcnt vmcnt(0)" ::: "memory");
  if (lane == 0) __hip_atomic_fetch_add((unsigned*)cnt, 1u, __ATOMIC_RELAXED, __HIP_MEMORY_SCOPE_AGENT);
}

__global__ __launch_bounds__(512) void k2_attn(char* __restrict__ ws, char* __restrict__ out) {
  extern __shared__ __attribute__((aligned(16))) char smem[];
  const int tid = threadIdx.x, lane = tid & 63;
  const int wave = __builtin_amdgcn_readfirstlane(tid >> 6);
  STAMP_DECL; STAMP(0);
  const int b = blockIdx.x;
  const int rblk = (b & 7) * 32 + (b >> 3);
  const int row = rblk * 8 + wave;
  const int c32 = lane & 31, h = lane >> 5;
  const unsigned lds0 = (unsigned)(uintptr_t)smem;
  const unsigned ctl = lds0 + L_CTL;
  const int ko = lane * 16;
  auto kv_fetch = [&](int i0, int n) {
#pragma unroll
    for (int i = i0; i < i0 + n; ++i) glds16(ko, ws + WS_KV + (wave + 8 * i) * 1024, lds0 + (wave + 8 * i) * 1024);
  };
  bf16x8 qf;
  unsigned qrem;
  {
    const char* qp = ws + WS_QB + (size_t)row * ROWB + c32 * 36;
    asm volatile("global_load_dwordx4 %0, %1, off" : "=v"(qf) : "v"(qp + h * 16) : "memory");
    asm volatile("global_load_dword %0, %1, off offset:32" : "=v"(qrem) : "v"(qp) : "memory");
    kv_fetch(0, 5);
    if (wave < 4) { const int o2 = tid * 128 < K2_CODE ? tid * 128 : 0; CODE_PREFETCH(K2_SYM, o2, ctl + 64); }
  }
  if (tid < 16) *(unsigned*)(smem + L_CTL + tid * 4) = 0u;
  const int vrow = c32 < 19 ? c32 : (c32 == 28 ? 18 : 19);
  const int kro = h ? V_OFF + 18 * 16 : KR_OFF + c32 * 16;
  const int vo = V_OFF + h * 320 + vrow * 16;
  const int rg = rblk / 9, cg = rblk - rg * 9;
  const int nrt = rg < 16 ? 5 : 4, rt0 = rg < 16 ? rg * 5 : 16 + rg * 4;
  const bool has_tile = rblk < 252;
  const int fr = lane & 15, kg = lane >> 4;
  const unsigned xo = (lane >> 3) * ROWB + (((lane & 7) ^ (lane >> 3)) << 4);
  auto fetch_tile = [&](const char* src) {
    const char* s0 = src + (size_t)(rt0 * 16 + wave * 8) * ROWB;
    const char* s1 = src + (size_t)(rt0 * 16 + 64 + (wave & 1) * 8) * ROWB;
    if (nrt == 5) {
#pragma unroll
      for (int c = 0; c < 9; ++c) {
        glds16_sc1(xo, s0 + c * 128, lds0 + c * XC_B + wave * 1024);
        if (c % 3 == 2) {
          const int ce = c == 8 ? 8 : (c / 3) * 4 + (wave >> 1);
          glds16_sc1(xo, s1 + ce * 128, lds0 + ce * XC_B + (8 + (wave & 1)) * 1024);
        }
      }
    } else {
#pragma unroll
      for (int c = 0; c < 9; ++c) glds16_sc1(xo, s0 + c * 128, lds0 + c * XC_B + wave * 1024);
    }
  };
  const char* cnt_p = ws + WS_CNT + ((has_tile ? rt0 : 0) + (lane < nrt ? lane : 0)) * 128;
  const char* wfa = ws + WS_WF + (size_t)((has_tile ? cg * 4 : 0) + (wave & 3)) * 18432 + (wave >> 2) * 1024;
  const f32x16 z = {};
  f32x16 o = z;
  u32x4 wA[9], wB[9];
  STAMP(1);
  asm volatile("s_waitcnt vmcnt(0)" : "+v"(qf), "+v"(qrem)::"memory");
  bar_lgkm();
  STAMP(2);
  kv_fetch(5, 7);
  {
          const char* srcr[3];
#pragma unroll
          for (int i = 0; i < 3; ++i) {
            int r = wave + 8 * i;
            r = r > 21 ? 21 : r;
            const int gr = rblk * 8 + r - 14;
            srcr[i] = ws + WS_QKV + (size_t)(gr < 0 ? 0 : gr) * (3 * ROWB) + ROWB;
            glds16(ko, srcr[i], lds0 + LA_0 + r * 2304);
            glds16(ko, srcr[i] + 1024, lds0 + LA_0 + r * 2304 + 1024);
          }
          const char* src = ws + WS_QKV + (size_t)row * (3 * ROWB);
          glds16(ko, src, lds0 + LA_Q + wave * ROWB);
#pragma unroll
          for (int i = 0; i < 3; ++i) {
            int r = wave + 8 * i;
            r = r > 21 ? 21 : r;
            glds16_part<0xffff>(ko, srcr[i] + 2048, lds0 + LA_0 + r * 2304 + 2048);
          }
          glds16_part<0xff>(ko, src + 1024, lds0 + LA_Q + wave * ROWB + 1024);
  }
  const float ms = max_pass(smem + ko, smem + kro, qf, rem_word(qrem, 0.f, h), 1);
  const unsigned rw = rem_word(qrem, bf_lo(pk2(ms + M_OFF, 0.f)), h);
  {
    int kq = ko, krq = kro, vq = vo;
    const char *kp = smem + kq, *krp = smem + krq, *vp = smem + vq;
    const s16x4 qe = mk2(rw, 0u), qo = mk2(0u, rw);
    s16x4 k01 = *(const s16x4*)krp, k23 = *(const s16x4*)(krp + 8);
    f32x16 sa = MF16(*(const bf16x8*)kp, qf, MF8(k01, qe, z)), sb;
    bf16x8 kf = *(const bf16x8*)(kp + 1024), v0 = *(const bf16x8*)vp, v1 = v0;
    u32x4 p0 = {}, p1 = {};
    unsigned cnt_seen;
    STAMP(3); CSTAMP(10);
#define WF9(W, BASE) asm volatile( \
        "global_load_dwordx4 %0, %9, %10\n\tglobal_load_dwordx4 %1, %9, %10 offset:2048\n\t" \
        "global_load_dwordx4 %2, %11, %10 offset:-4096\n\tglobal_load_dwordx4 %3, %11, %10 offset:-2048\n\t" \
        "global_load_dwordx4 %4, %11, %10\n\tglobal_load_dwordx4 %5, %11, %10 offset:2048\n\t" \
        "global_load_dwordx4 %6, %12, %10 offset:-4096\n\tglobal_load_dwordx4 %7, %12, %10 offset:-2048\n\t" \
        "global_load_dwordx4 %8, %12, %10" \
        : "=&v"(W[0]), "=&v"(W[1]), "=&v"(W[2]), "=&v"(W[3]), "=&v"(W[4]), "=&v"(W[5]), "=&v"(W[6]), "=&v"(W[7]), "=&v"(W[8]) \
        : "v"(ko), "s"(BASE), "v"(ko + 8192), "v"(ko + 16384) : "memory")
    auto group = [&](auto gc) __attribute__((always_inline)) {
      constexpr int g = decltype(gc)::value;
      LSTAMP(2 * g);
      if constexpr (PRIO(g) != PRIO(g - 1)) __builtin_amdgcn_s_setprio(PRIO(g));
      if constexpr (g == 1) { wait_vm<11>(); lds_add(ctl + C_KVA); }
      if constexpr (g == 2) { wait_vm<0>(); lds_add(ctl + C_AROWS); }
      if constexpr (g == 3) lds_wait(ctl + C_KVA, 8);
      if constexpr (g == 3 || g == 4) {
        if ((wave >> 2) == 4 - g) {
          lds_wait(ctl + C_AROWS, 8);
          local_attn(smem, lds0, wave, c32, h, row);
          store_row(smem + LA_Q + wave * ROWB, ws + WS_OA + (size_t)row * ROWB, lane);
          lds_add(ctl + C_A2);
        }
      }
      if constexpr (g == 6) {
        signal_row(ws + WS_CNT + (row >> 4) * 128, lane);
        lds_wait(ctl + C_A2, 8);
        kv_fetch(12, 7);
      }
      if constexpr (g == 7) { wait_vm<0>(); lds_add(ctl + C_KVB); }
      if constexpr (g == 8) WF9(wA, wfa);
      if constexpr (g == 9) lds_wait(ctl + C_KVB, 8);
      if constexpr (g == 10) WF9(wB, wfa + 36 * 18432);
      if constexpr (g == 11) lds_add(ctl + C_PAST);
      if constexpr (g == 12) asm volatile("global_load_dword %0, %1, off sc1" : "=v"(cnt_seen) : "v"(cnt_p) : "memory");
      if constexpr (g == 13) {
        lds_wait(ctl + C_PAST, 8);
        asm volatile("s_waitcnt vmcnt(0)" : "+v"(cnt_seen)::"memory");
        if (has_tile) {
          if (!__all(cnt_seen >= 16u)) cnt_wait(cnt_p);
          fetch_tile(ws + WS_OA);
        }
      }
      if constexpr (g == 15) { wait_vm<0>(); lds_add(ctl + C_XA); }
      LSTAMP(2 * g + 1);
      SB;
      const int G = (g & 3) * GRP_B;
      phase<true>(sa, sb, k01, krp + G + GRP_B, qo, kf, kp + G + 2048, qf, v0, vp + G + VT_B, v1, vp + G + 640, p0, p1, o);
      phase<false>(sb, sa, k23, nullptr, qe, kf, kp + G + 3072, qf, v0, vp + G + 2 * VT_B, v1, vp + G + VT_B + 640, p0, p1, o);
      phase<true>(sa, sb, k23, krp + G + GRP_B + 8, qo, kf, kp + G + GRP_B, qf, v0, vp + G + 3 * VT_B, v1, vp + G + 2 * VT_B + 640, p0, p1, o);
      phase<false>(sb, sa, k01, nullptr, qe, kf, kp + G + GRP_B + 1024, qf, v0, vp + G + GRP_B, v1, vp + G + 3 * VT_B + 640, p0, p1, o);
      asm volatile("" : "+v"(o), "+v"(sa), "+v"(p1));
      if constexpr ((g & 3) == 3) {
        kq += 4 * GRP_B; krq += 4 * GRP_B; vq += 4 * GRP_B;
        asm volatile("" : "+v"(kq), "+v"(krq), "+v"(vq));
        kp = smem + kq; krp = smem + krq; vp = smem + vq;
      }
    };
#define GR(i) group(IC<i>{});
    GR(0) GR(1) GR(2) GR(3) GR(4) GR(5) GR(6) GR(7) GR(8) GR(9) GR(10) GR(11) GR(12) GR(13) GR(14) GR(15)
    o = MF16(v1, __builtin_bit_cast(bf16x8, p1), o);
  }
  STAMP(4); CSTAMP(11); LSTAMP(32);
  lds_add(ctl + C_DONE);
  float l = h ? o[12] : o[10];
  float big = l;
#pragma unroll
  for (int i = 0; i < 10; ++i) big = fmaxf(big, fabsf(o[i]));
  bool ok = (big < 1.0e37f) && (l > 0.f);
#pragma unroll
  for (int i = 0; i < 10; ++i) ok = ok && (o[i] == o[i]);
  if (!__all(ok)) {
    attn_exact(ws + WS_KV, ko, kro, vo, qf, qrem, h, o);
    l = h ? o[12] : o[10];
  }
  {
    const float inv = __builtin_amdgcn_rcpf(l);
    unsigned* dst = (unsigned*)(smem + L_OB + wave * ROWB + c32 * 36 + h * 8);
    dst[0] = pk2(o[0] * inv, o[1] * inv);
    dst[1] = pk2(o[2] * inv, o[3] * inv);
    dst[4] = pk2(o[4] * inv, o[5] * inv);
    dst[5] = pk2(o[6] * inv, o[7] * inv);
    if (!h) dst[8] = pk2(o[8] * inv, o[9] * inv);
    store_row(smem + L_OB + wave * ROWB, ws + WS_OB + (size_t)row * ROWB, lane);
  }
  char* cntB = ws + WS_CNT + (128 + (row >> 4)) * 128;
  if (!has_tile) { signal_row(cntB, lane); return; }
  const int xa = fr * 128 + ((((wave >> 2) * 4 + kg) ^ (fr & 7)) << 4);
  char* pw = smem + L_P + (wave >> 2) * P_HALF + fr * P_LD + (wave & 3) * 64 + kg * 16;
  f32x4 acc[5];
  auto mm3 = [&](u32x4 (&w)[9], int c0) {
#pragma unroll
    for (int c = c0; c < c0 + 3; ++c) {
      bf16x8 xf[5];
#pragma unroll
      for (int m = 0; m < 5; ++m) xf[m] = *(const bf16x8*)(smem + c * XC_B + xa + m * 2048);
#pragma unroll
      for (int m = 0; m < 5; ++m) acc[m] = MFMA16(__builtin_bit_cast(bf16x8, w[c]), xf[m], c ? acc[m] : f32x4{});
    }
  };
  auto put = [&] {
#pragma unroll
    for (int m = 0; m < 5; ++m) *(f32x4*)(pw + m * 16 * P_LD) = acc[m];
  };
  auto tile_out = [&](int br) {
#pragma unroll
    for (int i = 0; i < 2; ++i) {
      const int r = (tid >> 3) + 64 * i, c = tid & 7;
      if (r < nrt * 16) {
        const char* p = smem + L_P + r * P_LD + c * 32;
        const f32x4 a0 = *(const f32x4*)p, a1 = *(const f32x4*)(p + 16);
        const f32x4 b0 = *(const f32x4*)(p + P_HALF), b1 = *(const f32x4*)(p + P_HALF + 16);
        const u32x4 u = {pk2(a0[0] + b0[0], a0[1] + b0[1]), pk2(a0[2] + b0[2], a0[3] + b0[3]),
                         pk2(a1[0] + b1[0], a1[1] + b1[1]), pk2(a1[2] + b1[2], a1[3] + b1[3])};
        ST16(out + (size_t)(rt0 * 16 + r) * (2 * ROWB) + br * ROWB + cg * 128 + c * 16, u);
      }
    }
  };
  signal_row(cntB, lane);
  STAMP(5);
  lds_wait(ctl + C_XA, 8);
  mm3(wA, 0);
  mm3(wA, 3);
  mm3(wA, 6);
  lds_wait(ctl + C_DONE, 8);
  put();
  if (wave == 0) cnt_wait(cnt_p + 128 * 128);
  bar_lgkm();
  STAMP(6);
  fetch_tile(ws + WS_OB);
  tile_out(0);
  if (nrt == 5) wait_vm<8>(); else wait_vm<6>();
  bar_lgkm();
  STAMP(7);
  mm3(wB, 0);
  if (nrt == 5) wait_vm<4>(); else wait_vm<3>();
  bar_lgkm();
  mm3(wB, 3);
  wait_vm<0>();
  bar_lgkm();
  mm3(wB, 6);
  put();
  bar_lgkm();
  STAMP(8);
  tile_out(1);
  STAMP(9);
  STAMP_OUT(1, ws); LSTAMP_OUT(ws);
}

extern "C" void kernel_launch(void* const* d_in, const int* in_sizes, int n_in, void* d_out, int out_size,
                              void* d_ws, size_t ws_size, hipStream_t stream) {
  (void)in_sizes; (void)n_in; (void)out_size; (void)ws_size;
  (void)hipFuncSetAttribute((const void*)k1_proj, hipFuncAttributeMaxDynamicSharedMemorySize, K1_LDS);
  (void)hipFuncSetAttribute((const void*)k2_attn, hipFuncAttributeMaxDynamicSharedMemorySize, K2_LDS_ALL);
  char* ws = (char*)d_ws;
  k1_proj<<<256, 512, K1_LDS, stream>>>((const char*)d_in[0], (const char*)d_in[1], (const char*)d_in[3], (const char*)d_in[4],
                                        (const char*)d_in[5], (const char*)d_in[6], (const char*)d_in[2], (const char*)d_in[7], ws);
  k2_attn<<<256, 512, K2_LDS_ALL, stream>>>(ws, (char*)d_out);
}
```

```cpp
#include <hip/hip_runtime.h>
#include <hip/hip_bf16.h>
#include <stdint.h>

typedef __bf16 bf16x2 __attribute__((ext_vector_type(2)));
typedef __bf16 bf16x8 __attribute__((ext_vector_type(8)));
typedef short s16x4 __attribute__((ext_vector_type(4)));
typedef float f32x2 __attribute__((ext_vector_type(2)));
typedef float f32x4 __attribute__((ext_vector_type(4)));
typedef float f32x16 __attribute__((ext_vector_type(16)));
typedef unsigned u32x2 __attribute__((ext_vector_type(2)));
typedef unsigned u32x4 __attribute__((ext_vector_type(4)));
#define LDSP(T) __attribute__((address_space(3))) T*
#define DI __device__ __forceinline__
#define STAMP_DECL
#define STAMP(i) do {} while (0)
#define CSTAMP(i) do {} while (0)
#define LSTAMP(i) do {} while (0)
#define LSTAMP_OUT(w) do {} while (0)
#define STAMP_ARG
#define STAMP_PASS
#define STAMP_OUT(kid, wsp) do {} while (0)

#define K1UNROLL _Pragma("unroll")
#define PRIO(g) ((g) < 0 ? -1 : 3 - (((g) >> 1) & 3))
constexpr int SEQ = 2048, DM = 576;
constexpr int ROWB = DM * 2;
constexpr float LOG2E = 1.4426950408889634f;
constexpr float QSCALE = 0.23570226039551584f * LOG2E;

constexpr int GRP_B = 9728, KR_OFF = 4096, V_OFF = 4608, VT_B = 1280;
constexpr int KV_BYTES = 16 * GRP_B;
constexpr size_t WS_KV = 0;
constexpr size_t WS_QB = WS_KV + KV_BYTES;
constexpr size_t WS_QKV = WS_QB + (size_t)SEQ * ROWB;
constexpr size_t WS_OA = WS_QKV + (size_t)SEQ * 3 * ROWB;
constexpr size_t WS_OB = WS_OA + (size_t)SEQ * ROWB;
constexpr size_t WS_WF = WS_OB + (size_t)SEQ * ROWB;
constexpr size_t WS_CNT = WS_WF + (size_t)72 * 18432;

template <int N> struct IC { static constexpr int value = N; };
DI unsigned pk2(float a, float b) {
  f32x2 f = {a, b};
  return __builtin_bit_cast(unsigned, __builtin_convertvector(f, bf16x2));
}
DI float dot2(unsigned a, unsigned b, float c) {
  return __builtin_amdgcn_fdot2_f32_bf16(__builtin_bit_cast(bf16x2, a), __builtin_bit_cast(bf16x2, b), c, false);
}
DI float bf_lo(unsigned u) { return __uint_as_float(u << 16); }
DI float bf_hi(unsigned u) { return __uint_as_float(u & 0xffff0000u); }

DI void glds16(unsigned voff, const void* sbase, unsigned lds_dst) {
  unsigned keep;
  asm volatile("s_mov_b32 %0, m0\n\ts_mov_b32 m0, %3\n\ts_nop 0\n\tglobal_load_lds_dwordx4 %1, %2\n\ts_mov_b32 m0, %0"
               : "=&s"(keep) : "v"(voff), "s"(sbase), "s"(lds_dst) : "memory");
}
DI void glds4(unsigned voff, const void* sbase, unsigned lds_dst) {
  unsigned keep;
  asm volatile("s_mov_b32 %0, m0\n\ts_mov_b32 m0, %3\n\ts_nop 0\n\tglobal_load_lds_dword %1, %2\n\ts_mov_b32 m0, %0"
               : "=&s"(keep) : "v"(voff), "s"(sbase), "s"(lds_dst) : "memory");
}
template <int N> DI void wait_vm() { asm volatile("s_waitcnt vmcnt(%0)" ::"i"(N) : "memory"); }
DI void bar_lgkm() { asm volatile("s_waitcnt lgkmcnt(0)\n\ts_barrier" ::: "memory"); }
DI bf16x8 tr_frag(const char* p) {
  s16x4 lo = __builtin_amdgcn_ds_read_tr16_b64_v4i16((LDSP(s16x4))(p));
  s16x4 hi = __builtin_amdgcn_ds_read_tr16_b64_v4i16((LDSP(s16x4))(p + 256));
  return __builtin_bit_cast(bf16x8, __builtin_shufflevector(lo, hi, 0, 1, 2, 3, 4, 5, 6, 7));
}
#define ST16(p, v) __builtin_nontemporal_store(v, (u32x4*)(p))
#define MFMA16(a, b, c) __builtin_amdgcn_mfma_f32_16x16x32_bf16(a, b, c, 0, 0, 0)

constexpr int K1_XB = 20480, K1_STAGE = 36864, K1_LDS = 4 * K1_STAGE;
constexpr int K1_RB = 13, K1_ACT = 14, K1_BCT = 5, K1_GEMM = K1_RB * (K1_ACT + K1_BCT);

template <int MODE>
DI void k1_loader(const char* X, int row0, const char* W0, const char* W1, int ldw, int colB0, int colB1,
                  const char* wk, const char* wv, int rot, unsigned lds0, int l, int lane) {
  unsigned xo[5], wo[4], eo[8];
  if (MODE != 2) {
#pragma unroll
    for (int i = 0; i < 5; ++i) {
      int r = row0 + (l + 4 * i) * 8 + (lane >> 3);
      r = r > SEQ - 1 ? SEQ - 1 : r;
      xo[i] = r * ROWB + (((lane & 7) ^ (lane >> 3)) << 4);
    }
  }
#pragma unroll
  for (int i = 0; i < 4; ++i) {
    const int kp = l + 4 * (i >> 1);
    const int k = kp * 8 + ((lane >> 2) & 7);
    wo[i] = k * ldw + ((i & 1) ? colB1 : colB0) + (lane >> 5) * 64 + (((lane & 3) ^ ((kp & 1) << 1)) << 4);
  }
  if (MODE == 1) {
#pragma unroll
    for (int i = 0; i < 4; ++i) {
      const int kp = l + 4 * (i >> 1);
      const int k = kp * 8 + (i & 1) * 4 + (lane >> 4);
      int dw = (lane & 15) ^ ((kp & 1) << 3);
      dw = dw > 8 ? 8 : dw;
      eo[i] = k * 36 + dw * 4;
    }
  }
  auto issue = [&](int j) {
    int ks = j + rot; ks = ks >= 9 ? ks - 9 : ks;
    const unsigned sb = lds0 + (j & 3) * K1_STAGE;
    if (MODE != 2) {
      const char* xb = X + ks * 128;
#pragma unroll
      for (int i = 0; i < 5; ++i) glds16(xo[i], xb, sb + (l + 4 * i) * 1024);
    }
    const char* w0 = W0 + (size_t)ks * 64 * ldw;
    const char* w1 = W1 + (size_t)ks * 64 * ldw;
#pragma unroll
    for (int i = 0; i < 4; ++i) {
      if (MODE == 1 && (i & 1)) continue;
      glds16(wo[i], (i & 1) ? w1 : w0, sb + K1_XB + (l + 4 * (i >> 1)) * 2048 + (i & 1) * 1024);
    }
    if (MODE == 1) {
      const char* kb = wk + ks * (64 * 36);
      const char* vb = wv + ks * (64 * 36);
#pragma unroll
      for (int i = 0; i < 4; ++i) {
        const unsigned d = sb + K1_XB + (l + 4 * (i >> 1)) * 2048 + 1024 + (i & 1) * 256;
        glds4(eo[i], kb, d);
        glds4(eo[i], vb, d + 512);
      }
    }
  };
  constexpr int P = MODE == 0 ? 9 : (MODE == 1 ? 15 : 4);
#pragma unroll 1
  for (int t = 0; t < 9; ++t) {
    issue(t);
    if (t == 1) { wait_vm<P>(); bar_lgkm(); }
    else if (t >= 3) { wait_vm<2 * P>(); bar_lgkm(); }
  }
  wait_vm<P>(); bar_lgkm();
  wait_vm<0>(); bar_lgkm();
  bar_lgkm();
}

__global__ __launch_bounds__(512) void k1_proj(const char* __restrict__ xA, const char* __restrict__ wqkv,
                                               const char* __restrict__ xB, const char* __restrict__ wq,
                                               const char* __restrict__ wk, const char* __restrict__ wv,
                                               const char* __restrict__ woA, const char* __restrict__ woB,
                                               char* __restrict__ ws) {
  extern __shared__ __attribute__((aligned(16))) char smem[];
  const int tid = threadIdx.x, lane = tid & 63;
  const int wave = __builtin_amdgcn_readfirstlane(tid >> 6);
  STAMP_DECL; STAMP(0);
  const unsigned lds0 = (unsigned)(uintptr_t)smem;
  const int b = blockIdx.x;
  const int unit = (b & 7) * 32 + (b >> 3);
  const int fr = lane & 15, kg = lane >> 4;
  const int rot = unit % 9;
  const int tro = kg * 2048 + (fr >> 2) * 64 + (fr & 3) * 8;
  const int trE = K1_XB + tro + ((kg & 1) << 5), trO = K1_XB + tro + (((kg & 1) ^ 1) << 5);

  if (unit >= K1_GEMM) {
    const int s = unit - K1_GEMM;
    if (s == 0 && tid < 256) *(unsigned*)(ws + WS_CNT + tid * 128) = 0u;
    if (wave >= 4) {
      const int c0 = s * 128, c1 = c0 + 64;
      k1_loader<2>(nullptr, 0, c0 < DM ? woA : woB, c1 < DM ? woA : woB, ROWB, (c0 < DM ? c0 : c0 - DM) * 2, (c1 < DM ? c1 : c1 - DM) * 2,
                   nullptr, nullptr, rot, lds0, wave - 4, lane);
    } else {
      bar_lgkm();
      char* dst = ws + WS_WF + (size_t)(s * 8 + wave * 2) * 18432 + lane * 16;
#pragma unroll 1
      for (int j = 0; j < 9; ++j) {
        int ks = j + rot; ks = ks >= 9 ? ks - 9 : ks;
        const char* sb = smem + (j & 3) * K1_STAGE + wave * 512;
        const bf16x8 f0 = tr_frag(sb + trE), f1 = tr_frag(sb + trO), f2 = tr_frag(sb + trE + 8192), f3 = tr_frag(sb + trO + 8192);
        bar_lgkm();
        ST16(dst + ks * 2048, __builtin_bit_cast(u32x4, f0));
        ST16(dst + ks * 2048 + 18432, __builtin_bit_cast(u32x4, f1));
        ST16(dst + ks * 2048 + 1024, __builtin_bit_cast(u32x4, f2));
        ST16(dst + ks * 2048 + 18432 + 1024, __builtin_bit_cast(u32x4, f3));
      }
    }
    STAMP(9);
    STAMP_OUT(0, ws);
    return;
  }
  const bool isA = unit < K1_RB * K1_ACT;
  int rb, ct;
  if (isA) {
    if (unit < 70) { ct = unit / 5; rb = unit - ct * 5; }
    else { const int u = unit - 70, band = u >= 56 ? 1 : 0, v = u - band * 56; ct = v >> 2; rb = 5 + band * 4 + (v & 3); }
  } else { const int u = unit - K1_RB * K1_ACT; rb = u / K1_BCT; ct = u - rb * K1_BCT; }
  const int row0 = rb * 160;
  const bool last = ct == (isA ? K1_ACT - 1 : K1_BCT - 1);
  const int wm = wave & 1, wn = (wave >> 1) & 1;
  constexpr int OT_LD = 272;
  if (wave >= 4) {
    if (isA) k1_loader<0>(xA, row0, wqkv, wqkv, 3 * ROWB, ct * 256, ct * 256 + (last ? 0 : 128), nullptr, nullptr, rot, lds0, wave - 4, lane);
    else if (!last) k1_loader<0>(xB, row0, wq, wq, ROWB, ct * 256, ct * 256 + 128, nullptr, nullptr, rot, lds0, wave - 4, lane);
    else k1_loader<1>(xB, row0, wq, wq, ROWB, ct * 256, 0, wk, wv, rot, lds0, wave - 4, lane);
  } else {
    f32x4 acc[5][4] = {};
    const int xo0 = (wm * 80 + fr) * 128 + ((kg ^ (fr & 7)) << 4), xo1 = xo0 ^ 64;
    const int wE = trE + wn * 1024, wO = trO + wn * 1024;
    bf16x8 xa[5], wa[4], xb[5], wb[4];
    auto rd = [&](const char* sb, int kk, bf16x8 (&xf)[5], bf16x8 (&wf)[4]) {
#pragma unroll
      for (int m = 0; m < 5; ++m) xf[m] = *(const bf16x8*)(sb + (kk ? xo1 : xo0) + m * 2048);
#pragma unroll
      for (int n = 0; n < 4; ++n) wf[n] = tr_frag(sb + ((n & 1) ? wO : wE) + kk * 8192 + (n >> 1) * 512);
    };
    auto mm = [&](bf16x8 (&xf)[5], bf16x8 (&wf)[4]) {
#pragma unroll
      for (int n = 0; n < 4; ++n)
#pragma unroll
        for (int m = 0; m < 5; ++m) acc[m][n] = MFMA16(wf[n], xf[m], acc[m][n]);
    };
    bar_lgkm();
    STAMP(1); CSTAMP(10);
    rd(smem, 0, xa, wa);
#define ILV() do { _Pragma("unroll") for (int q_ = 0; q_ < 13; ++q_) { __builtin_amdgcn_sched_group_barrier(0x8, 1, 0); __builtin_amdgcn_sched_group_barrier(0x100, 1, 0); } \
                   __builtin_amdgcn_sched_group_barrier(0x8, 7, 0); } while (0)
    K1UNROLL
    for (int j = 0; j < 9; ++j) {
      rd(smem + (j & 3) * K1_STAGE, 1, xb, wb);
      mm(xa, wa);
      ILV();
      __builtin_amdgcn_sched_barrier(0);
      bar_lgkm();
      rd(smem + ((j + 1) & 3) * K1_STAGE, 0, xa, wa);
      mm(xb, wb);
      ILV();
      __builtin_amdgcn_sched_barrier(0);
    }
    STAMP(2); CSTAMP(11);
    const bool scale = !isA && !(last && wn == 1);
#pragma unroll
    for (int m = 0; m < 5; ++m)
#pragma unroll
      for (int n = 0; n < 4; ++n) {
        u32x2 u = {pk2(acc[m][n][0], acc[m][n][1]), pk2(acc[m][n][2], acc[m][n][3])};
        if (scale) {
          u[0] = pk2(bf_lo(u[0]) * QSCALE, bf_hi(u[0]) * QSCALE);
          u[1] = pk2(bf_lo(u[1]) * QSCALE, bf_hi(u[1]) * QSCALE);
        }
        *(u32x2*)(smem + (wm * 80 + m * 16 + fr) * OT_LD + wn * 128 + n * 32 + kg * 8) = u;
      }
  }
  bar_lgkm();
  STAMP(3);
  {
    char* dst = isA ? ws + WS_QKV : ws + WS_QB;
    const int ld = isA ? 3 * ROWB : ROWB;
    const int c = tid & 15;
    if (!(last && c >= 8)) {
#pragma unroll
      for (int i = 0; i < 5; ++i) {
        const int r = (tid >> 4) + 32 * i;
        if (row0 + r < SEQ) ST16(dst + (size_t)(row0 + r) * ld + ct * 256 + c * 16, *(const u32x4*)(smem + r * OT_LD + c * 16));
      }
    }
  }
  if (!isA && last) {
    char* kv = ws + WS_KV;
    if (tid < 320) {
      const int r = tid >> 1, hh = tid & 1, key = row0 + r;
      if (key < SEQ) {
        const int t = key >> 5, k32 = key & 31;
        char* g = kv + (t >> 2) * GRP_B;
        *(u32x4*)(g + (t & 3) * 1024 + (hh * 32 + k32) * 16) = *(const u32x4*)(smem + r * OT_LD + 128 + hh * 16);
        if (hh == 0) *(unsigned*)(g + KR_OFF + k32 * 16 + (t & 3) * 4) = *(const unsigned*)(smem + r * OT_LD + 160);
      }
    }
    if (tid < 400) {
      const int blk = tid / 20, d = tid - blk * 20;
      const int tl = blk >> 2, s = (blk >> 1) & 1, hh = blk & 1;
      const int t = (row0 >> 5) + tl;
      if (t < 64) {
        u32x4 o;
        if (d < 18) {
          const char* src = smem + (tl * 32 + s * 16 + hh * 4) * OT_LD + 192 + d * 2;
#pragma unroll
          for (int e = 0; e < 4; ++e) {
            const int r = (e >> 1) * 8 + (e & 1) * 2;
            o[e] = (unsigned)*(const unsigned short*)(src + r * OT_LD) | ((unsigned)*(const unsigned short*)(src + (r + 1) * OT_LD) << 16);
          }
        } else {
          const unsigned f = d == 18 ? 0x3f803f80u : 0u;
          o = u32x4{f, f, f, f};
        }
        *(u32x4*)(kv + (t >> 2) * GRP_B + V_OFF + (t & 3) * VT_B + (s * 2 + hh) * 320 + d * 16) = o;
      }
    }
  }
  STAMP(9);
  STAMP_OUT(0, ws);
}

constexpr int K2_LDS = KV_BYTES + 6144;
constexpr float M_OFF = 96.0f;
#define SB __builtin_amdgcn_sched_barrier(0)
#define MF16(a, b, c) __builtin_amdgcn_mfma_f32_32x32x16_bf16(a, b, c, 0, 0, 0)
#define MF8(a, b, c) __builtin_amdgcn_mfma_f32_32x32x8bf16_1k(a, b, c, 0, 0, 0)
#define EX(i) cur[i] = __builtin_amdgcn_exp2f(cur[i])

DI float xhalf(float x);
DI float max16(const f32x16& s, float m) {
#pragma unroll
  for (int i = 0; i < 16; i += 2) m = __builtin_fmaxf(__builtin_fmaxf(s[i], s[i + 1]), m);
  return m;
}
#define E(i) EX(i); SB;
#define C0(i) p0[i] = pk2(cur[2 * i], cur[2 * i + 1]); SB;
#define C1(i) p1[i] = pk2(cur[8 + 2 * i], cur[9 + 2 * i]); SB;
template <bool RELOAD>
DI void phase(f32x16& cur, f32x16& nxt, s16x4& kra, const char* krp, const s16x4 qr, bf16x8& kf, const char* kp2, const bf16x8 qf,
              bf16x8& v0, const char* v0p, bf16x8& v1, const char* v1p, u32x4& p0, u32x4& p1, f32x16& o) {
  const f32x16 z = {};
  nxt = MF8(kra, qr, z); SB;
  if (RELOAD) { kra = *(const s16x4*)krp; SB; }
  E(0) E(1) E(2) E(3) C0(0) E(4)
  o = MF16(v1, __builtin_bit_cast(bf16x8, p1), o); SB;
  v1 = *(const bf16x8*)v1p; SB;
  C0(1) E(5) E(6) E(7) C0(2) E(8)
  nxt = MF16(kf, qf, nxt); SB;
  kf = *(const bf16x8*)kp2; SB;
  C0(3) E(9) E(10) E(11) C1(0) E(12)
  o = MF16(v0, __builtin_bit_cast(bf16x8, p0), o); SB;
  v0 = *(const bf16x8*)v0p; SB;
  C1(1) E(13) E(14) E(15) C1(2) C1(3)
}
DI unsigned rem_word(unsigned qrem, float m, int h) {
  const unsigned hi = pk2(-m, 0.f) & 0xffffu;
  const unsigned lo = pk2(-m - bf_lo(hi), 0.f) & 0xffffu;
  return h ? (hi | (lo << 16)) : qrem;
}
DI s16x4 mk2(unsigned a, unsigned b) { u32x2 r = {a, b}; return __builtin_bit_cast(s16x4, r); }

DI float max_pass(const char* kp, const char* krp, const bf16x8 qf, unsigned rw, int npair) {
  const s16x4 qe = mk2(rw, 0u), qo = mk2(0u, rw);
  const f32x16 z = {};
  float m = -3.0e38f;
  for (int i = 0; i < npair; ++i) {
    const int G = (i >> 1) * GRP_B, hf = i & 1;
    const s16x4 kr = *(const s16x4*)(krp + G + hf * 8);
    m = max16(MF16(*(const bf16x8*)(kp + G + hf * 2048), qf, MF8(kr, qe, z)), m);
    m = max16(MF16(*(const bf16x8*)(kp + G + hf * 2048 + 1024), qf, MF8(kr, qo, z)), m);
  }
  return fmaxf(m, xhalf(m));
}
DI void attn_exact(const char* img, int ko, int kro, int vo, const bf16x8 qf, unsigned qrem, int h, f32x16& o) {
  const float mt = max_pass(img + ko, img + kro, qf, rem_word(qrem, 0.f, h), 32);
  const unsigned rw = rem_word(qrem, mt, h);
  const s16x4 qe = mk2(rw, 0u), qo = mk2(0u, rw);
  const f32x16 z = {};
  o = z;
  for (int t = 0; t < 64; ++t) {
    const char* g = img + (t >> 2) * GRP_B;
    const s16x4 kr = *(const s16x4*)(g + kro + (t & 2) * 4);
    f32x16 cur = MF16(*(const bf16x8*)(g + ko + (t & 3) * 1024), qf, MF8(kr, (t & 1) ? qo : qe, z));
    u32x4 p0, p1;
#pragma unroll
    for (int i = 0; i < 16; ++i) EX(i);
#pragma unroll
    for (int i = 0; i < 4; ++i) { p0[i] = pk2(cur[2 * i], cur[2 * i + 1]); p1[i] = pk2(cur[8 + 2 * i], cur[9 + 2 * i]); }
    o = MF16(*(const bf16x8*)(g + vo + (t & 3) * VT_B), __builtin_bit_cast(bf16x8, p0), o);
    o = MF16(*(const bf16x8*)(g + vo + (t & 3) * VT_B + 640), __builtin_bit_cast(bf16x8, p1), o);
  }
}

constexpr int LA_0 = 98304;
constexpr int LA_Q = LA_0 + 22 * 2304;
constexpr int L_OB = 94208;
constexpr int L_P = 104448;
constexpr int P_LD = 272, P_HALF = 80 * P_LD;
constexpr int XC_B = 10240;
constexpr int L_CTL = K2_LDS;
constexpr int K2_LDS_ALL = K2_LDS + 64;
enum { C_KVA = 0, C_KVB = 4, C_PAST = 8, C_AROWS = 12, C_A2 = 16, C_XA = 20, C_DONE = 24, C_PROG = 32 };

DI void lds_add(unsigned addr) {
  asm volatile("s_waitcnt lgkmcnt(0)\n\ts_mov_b64 exec, 1\n\tds_add_u32 %0, %1\n\ts_mov_b64 exec, -1" ::"v"(addr), "v"(1u) : "memory");
}
DI void lds_wait(unsigned addr, unsigned n) {
  for (;;) {
    unsigned r;
    asm volatile("ds_read_b32 %0, %1\n\ts_waitcnt lgkmcnt(0)" : "=&v"(r) : "v"(addr) : "memory");
    if (__builtin_amdgcn_readfirstlane(r) >= n) break;
    __builtin_amdgcn_s_sleep(2);
  }
}
DI void cnt_wait(const char* p) {
  for (int spin = 0; spin < (1 << 22); ++spin) {
    unsigned r;
    asm volatile("global_load_dword %0, %1, off sc1\n\ts_waitcnt vmcnt(0)" : "=&v"(r) : "v"(p) : "memory");
    if (__all(r >= 16u)) break;
    __builtin_amdgcn_s_sleep(2);
  }
}
DI float xhalf(float x) {
  const unsigned u = __float_as_uint(x);
  const auto r = __builtin_amdgcn_permlane32_swap(u, u, false, false);
  return __uint_as_float((threadIdx.x & 32) ? r[0] : r[1]);
}
DI void local_attn(char* smem, unsigned lds0, int wave, int c32, int h, int row) {
  unsigned* qp = (unsigned*)(smem + LA_Q + wave * ROWB + c32 * 36);
  unsigned q[9];
#pragma unroll
  for (int e = 0; e < 9; ++e) q[e] = qp[e];
  const unsigned kb = lds0 + LA_0 + (wave + 8 - h * 8) * 2304 + c32 * 36;
  const unsigned vb = lds0 + LA_0 + wave * 2304 + ROWB + c32 * 36 + h * 16;
  u32x2 kk[9][2], vv[5][4];
#pragma unroll
  for (int e = 0; e < 9; ++e)
    asm volatile("ds_read2st64_b32 %0, %2 offset0:54 offset1:36\n\tds_read2st64_b32 %1, %2 offset0:18 offset1:0"
                 : "=&v"(kk[e][0]), "=&v"(kk[e][1]) : "v"(kb + e * 4) : "memory");
  asm volatile("s_waitcnt lgkmcnt(0)" : "+v"(kk[0][0]), "+v"(kk[0][1]), "+v"(kk[1][0]), "+v"(kk[1][1]), "+v"(kk[2][0]), "+v"(kk[2][1]),
      "+v"(kk[3][0]), "+v"(kk[3][1]), "+v"(kk[4][0]), "+v"(kk[4][1]), "+v"(kk[5][0]), "+v"(kk[5][1]), "+v"(kk[6][0]), "+v"(kk[6][1]),
      "+v"(kk[7][0]), "+v"(kk[7][1]), "+v"(kk[8][0]), "+v"(kk[8][1])::"memory");
#pragma unroll
  for (int e = 0; e < 5; ++e)
    asm volatile("ds_read2st64_b32 %0, %4 offset0:126 offset1:108\n\tds_read2st64_b32 %1, %4 offset0:90 offset1:72\n\t"
                 "ds_read2st64_b32 %2, %4 offset0:54 offset1:36\n\tds_read2st64_b32 %3, %4 offset0:18 offset1:0"
                 : "=&v"(vv[e][0]), "=&v"(vv[e][1]), "=&v"(vv[e][2]), "=&v"(vv[e][3]) : "v"(vb + e * 4) : "memory");
  float lg[4] = {0.f, 0.f, 0.f, 0.f};
#pragma unroll
  for (int e = 0; e < 9; ++e) {
    const unsigned k0 = kk[e][0][0], k1 = kk[e][0][1], k2 = kk[e][1][0], k3 = kk[e][1][1];
    lg[0] = dot2(q[e], k0, lg[0]); lg[1] = dot2(q[e], k1, lg[1]); lg[2] = dot2(q[e], k2, lg[2]); lg[3] = dot2(q[e], k3, lg[3]);
  }
#pragma unroll
  for (int w = 0; w < 4; ++w) lg[w] = row - 2 * (h * 4 + w) >= 0 ? lg[w] * QSCALE : -1.0e9f * LOG2E;
  float mx = fmaxf(fmaxf(lg[0], lg[1]), fmaxf(lg[2], lg[3]));
  mx = fmaxf(mx, xhalf(mx));
  float sum = 0.f;
#pragma unroll
  for (int w = 0; w < 4; ++w) { lg[w] = __builtin_amdgcn_exp2f(lg[w] - mx); sum += lg[w]; }
  sum += xhalf(sum);
  const float inv = __builtin_amdgcn_rcpf(sum);
  const unsigned pa = pk2(lg[0] * inv, lg[1] * inv), pb = pk2(lg[2] * inv, lg[3] * inv);
  const auto ra = __builtin_amdgcn_permlane32_swap(pa, pa, false, false);
  const auto rb = __builtin_amdgcn_permlane32_swap(pb, pb, false, false);
  const unsigned pp[4] = {ra[0], rb[0], ra[1], rb[1]};
  asm volatile("s_waitcnt lgkmcnt(0)" : "+v"(vv[0][0]), "+v"(vv[0][1]), "+v"(vv[0][2]), "+v"(vv[0][3]), "+v"(vv[1][0]), "+v"(vv[1][1]),
      "+v"(vv[1][2]), "+v"(vv[1][3]), "+v"(vv[2][0]), "+v"(vv[2][1]), "+v"(vv[2][2]), "+v"(vv[2][3]), "+v"(vv[3][0]), "+v"(vv[3][1]),
      "+v"(vv[3][2]), "+v"(vv[3][3]), "+v"(vv[4][0]), "+v"(vv[4][1]), "+v"(vv[4][2]), "+v"(vv[4][3])::"memory");
#pragma unroll
  for (int e = 0; e < 5; ++e) {
    float o0 = 0.f, o1 = 0.f;
#pragma unroll
    for (int w2 = 0; w2 < 4; ++w2) {
      const unsigned x = vv[e][w2][0], y = vv[e][w2][1];
      o0 = dot2(__builtin_amdgcn_perm(y, x, 0x05040100u), pp[w2], o0);
      o1 = dot2(__builtin_amdgcn_perm(y, x, 0x07060302u), pp[w2], o1);
    }
    qp[h * 4 + e] = pk2(o0, o1);
  }
}
template <int MASK> DI void glds16_part(unsigned voff, const void* sbase, unsigned lds_dst) {
  unsigned keep;
  asm volatile("s_mov_b32 %0, m0\n\ts_mov_b32 m0, %3\n\ts_mov_b64 exec, %4\n\tglobal_load_lds_dwordx4 %1, %2\n\ts_mov_b64 exec, -1\n\ts_mov_b32 m0, %0"
               : "=&s"(keep) : "v"(voff), "s"(sbase), "s"(lds_dst), "i"(MASK) : "memory");
}
DI void glds16_sc1(unsigned voff, const void* sbase, unsigned lds_dst) {
  unsigned keep;
  asm volatile("s_mov_b32 %0, m0\n\ts_mov_b32 m0, %3\n\ts_nop 0\n\tglobal_load_lds_dwordx4 %1, %2 sc1\n\ts_mov_b32 m0, %0"
               : "=&s"(keep) : "v"(voff), "s"(sbase), "s"(lds_dst) : "memory");
}
DI void store_row(const char* stage, char* dst, int lane) {
  asm volatile("s_waitcnt lgkmcnt(0)" ::: "memory");
  const u32x4 a = *(const u32x4*)(stage + lane * 16);
  const u32x4 b = *(const u32x4*)(stage + 1024 + (lane & 7) * 16);
  asm volatile("global_store_dwordx4 %0, %1, off sc1\n\ts_nop 1" ::"v"(dst + lane * 16), "v"(a) : "memory");
  if (lane < 8) asm volatile("global_store_dwordx4 %0, %1, off offset:1024 sc1\n\ts_nop 1" ::"v"(dst + lane * 16), "v"(b) : "memory");
}
DI void signal_row(char* cnt, int lane) {
  asm volatile("s_waitcnt vmcnt(0)" ::: "memory");
  if (lane == 0) __hip_atomic_fetch_add((unsigned*)cnt, 1u, __ATOMIC_RELAXED, __HIP_MEMORY_SCOPE_AGENT);
}

__global__ __launch_bounds__(512) void k2_attn(char* __restrict__ ws, char* __restrict__ out) {
  extern __shared__ __attribute__((aligned(16))) char smem[];
  const int tid = threadIdx.x, lane = tid & 63;
  const int wave = __builtin_amdgcn_readfirstlane(tid >> 6);
  STAMP_DECL; STAMP(0);
  const int b = blockIdx.x;
  const int rblk = (b & 7) * 32 + (b >> 3);
  const int row = rblk * 8 + wave;
  const int c32 = lane & 31, h = lane >> 5;
  const unsigned lds0 = (unsigned)(uintptr_t)smem;
  const unsigned ctl = lds0 + L_CTL;
  const int ko = lane * 16;
  auto kv_fetch = [&](int p0, int step, int n) {
#pragma unroll
    for (int i = 0; i < n; ++i) glds16(ko, ws + WS_KV + (p0 + step * i) * 1024, lds0 + (p0 + step * i) * 1024);
  };
  bf16x8 qf;
  unsigned qrem;
  {
    const char* qp = ws + WS_QB + (size_t)row * ROWB + c32 * 36;
    asm volatile("global_load_dwordx4 %0, %1, off" : "=v"(qf) : "v"(qp + h * 16) : "memory");
    asm volatile("global_load_dword %0, %1, off offset:32" : "=v"(qrem) : "v"(qp) : "memory");
    kv_fetch(wave, 8, 5);
  }
  if (tid < 16) *(unsigned*)(smem + L_CTL + tid * 4) = 0u;
  const int vrow = c32 < 19 ? c32 : (c32 == 28 ? 18 : 19);
  const int kro = h ? V_OFF + 18 * 16 : KR_OFF + c32 * 16;
  const int vo = V_OFF + h * 320 + vrow * 16;
  const int rg = rblk / 9, cg = rblk - rg * 9;
  const int nrt = rg < 16 ? 5 : 4, rt0 = rg < 16 ? rg * 5 : 16 + rg * 4;
  const bool has_tile = rblk < 252;
  const int fr = lane & 15, kg = lane >> 4;
  const unsigned xo = (lane >> 3) * ROWB + (((lane & 7) ^ (lane >> 3)) << 4);
  auto fetch_tile = [&](const char* src) {
    const char* s0 = src + (size_t)(rt0 * 16 + wave * 8) * ROWB;
    const char* s1 = src + (size_t)(rt0 * 16 + 64 + (wave & 1) * 8) * ROWB;
    if (nrt == 5) {
#pragma unroll
      for (int c = 0; c < 9; ++c) {
        glds16_sc1(xo, s0 + c * 128, lds0 + c * XC_B + wave * 1024);
        if (c % 3 == 2) {
          const int ce = c == 8 ? 8 : (c / 3) * 4 + (wave >> 1);
          glds16_sc1(xo, s1 + ce * 128, lds0 + ce * XC_B + (8 + (wave & 1)) * 1024);
        }
      }
    } else {
#pragma unroll
      for (int c = 0; c < 9; ++c) glds16_sc1(xo, s0 + c * 128, lds0 + c * XC_B + wave * 1024);
    }
  };
  const char* cnt_p = ws + WS_CNT + ((has_tile ? rt0 : 0) + (lane < nrt ? lane : 0)) * 128;
  const char* wfa = ws + WS_WF + (size_t)((has_tile ? cg * 4 : 0) + (wave & 3)) * 18432 + (wave >> 2) * 1024;
  const f32x16 z = {};
  f32x16 o = z;
  u32x4 wA[9], wB[9];
  STAMP(1);
  asm volatile("s_waitcnt vmcnt(0)" : "+v"(qf), "+v"(qrem)::"memory");
  bar_lgkm();
  STAMP(2);
  if (wave >= 4) {
    const int w4 = wave - 4;
    kv_fetch(40 + w4, 4, 14);
#pragma unroll
    for (int i = 0; i < 6; ++i) {
      int r = w4 + 4 * i;
      r = r > 21 ? 21 : r;
      const int gr = rblk * 8 + r - 14;
      const char* src = ws + WS_QKV + (size_t)(gr < 0 ? 0 : gr) * (3 * ROWB) + ROWB;
      glds16(ko, src, lds0 + LA_0 + r * 2304);
      glds16(ko, src + 1024, lds0 + LA_0 + r * 2304 + 1024);
      glds16_part<0xffff>(ko, src + 2048, lds0 + LA_0 + r * 2304 + 2048);
    }
#pragma unroll
    for (int i = 0; i < 2; ++i) {
      const char* src = ws + WS_QKV + (size_t)(rblk * 8 + w4 + 4 * i) * (3 * ROWB);
      glds16(ko, src, lds0 + LA_Q + (w4 + 4 * i) * ROWB);
      glds16_part<0xff>(ko, src + 1024, lds0 + LA_Q + (w4 + 4 * i) * ROWB + 1024);
    }
    wait_vm<22>();
    lds_add(ctl + C_KVA);
  }
  const float ms = max_pass(smem + ko, smem + kro, qf, rem_word(qrem, 0.f, h), 1);
  const unsigned rw = rem_word(qrem, bf_lo(pk2(ms + M_OFF, 0.f)), h);
  {
    int kq = ko, krq = kro, vq = vo;
    const char *kp = smem + kq, *krp = smem + krq, *vp = smem + vq;
    const s16x4 qe = mk2(rw, 0u), qo = mk2(0u, rw);
    s16x4 k01 = *(const s16x4*)krp, k23 = *(const s16x4*)(krp + 8);
    f32x16 sa = MF16(*(const bf16x8*)kp, qf, MF8(k01, qe, z)), sb;
    bf16x8 kf = *(const bf16x8*)(kp + 1024), v0 = *(const bf16x8*)vp, v1 = v0;
    u32x4 p0 = {}, p1 = {};
    unsigned cnt_seen;
    STAMP(3); CSTAMP(10);
#define WF9(W, BASE) asm volatile( \
        "global_load_dwordx4 %0, %9, %10\n\tglobal_load_dwordx4 %1, %9, %10 offset:2048\n\t" \
        "global_load_dwordx4 %2, %11, %10 offset:-4096\n\tglobal_load_dwordx4 %3, %11, %10 offset:-2048\n\t" \
        "global_load_dwordx4 %4, %11, %10\n\tglobal_load_dwordx4 %5, %11, %10 offset:2048\n\t" \
        "global_load_dwordx4 %6, %12, %10 offset:-4096\n\tglobal_load_dwordx4 %7, %12, %10 offset:-2048\n\t" \
        "global_load_dwordx4 %8, %12, %10" \
        : "=&v"(W[0]), "=&v"(W[1]), "=&v"(W[2]), "=&v"(W[3]), "=&v"(W[4]), "=&v"(W[5]), "=&v"(W[6]), "=&v"(W[7]), "=&v"(W[8]) \
        : "v"(ko), "s"(BASE), "v"(ko + 8192), "v"(ko + 16384) : "memory")
    auto group = [&](auto gc) __attribute__((always_inline)) {
      constexpr int g = decltype(gc)::value;
      LSTAMP(2 * g);
      if constexpr (PRIO(g) != PRIO(g - 1)) __builtin_amdgcn_s_setprio(PRIO(g));
      if constexpr (g == 1) { wait_vm<0>(); lds_add(ctl + C_AROWS); }
      if constexpr (g == 3) lds_wait(ctl + C_KVA, 4);
      if constexpr (g == 3 || g == 4) {
        if ((wave >> 2) == 4 - g) {
          lds_wait(ctl + C_AROWS, 8);
          local_attn(smem, lds0, wave, c32, h, row);
          store_row(smem + LA_Q + wave * ROWB, ws + WS_OA + (size_t)row * ROWB, lane);
          lds_add(ctl + C_A2);
        }
      }
      if constexpr (g == 6) {
        signal_row(ws + WS_CNT + (row >> 4) * 128, lane);
        lds_wait(ctl + C_A2, 8);
        kv_fetch(96 + wave, 8, 7);
      }
      if constexpr (g == 7) { wait_vm<0>(); lds_add(ctl + C_KVB); }
      if constexpr (g == 8) WF9(wA, wfa);
      if constexpr (g == 9) lds_wait(ctl + C_KVB, 8);
      if constexpr (g == 10) WF9(wB, wfa + 36 * 18432);
      if constexpr (g == 11) lds_add(ctl + C_PAST);
      if constexpr (g == 12) asm volatile("global_load_dword %0, %1, off sc1" : "=v"(cnt_seen) : "v"(cnt_p) : "memory");
      if constexpr (g == 13) {
        lds_wait(ctl + C_PAST, 8);
        asm volatile("s_waitcnt vmcnt(0)" : "+v"(cnt_seen)::"memory");
        if (has_tile) {
          if (!__all(cnt_seen >= 16u)) cnt_wait(cnt_p);
          fetch_tile(ws + WS_OA);
        }
      }
      if constexpr (g == 15) { wait_vm<0>(); lds_add(ctl + C_XA); }
      LSTAMP(2 * g + 1);
      SB;
      const int G = (g & 3) * GRP_B;
      phase<true>(sa, sb, k01, krp + G + GRP_B, qo, kf, kp + G + 2048, qf, v0, vp + G + VT_B, v1, vp + G + 640, p0, p1, o);
      phase<false>(sb, sa, k23, nullptr, qe, kf, kp + G + 3072, qf, v0, vp + G + 2 * VT_B, v1, vp + G + VT_B + 640, p0, p1, o);
      phase<true>(sa, sb, k23, krp + G + GRP_B + 8, qo, kf, kp + G + GRP_B, qf, v0, vp + G + 3 * VT_B, v1, vp + G + 2 * VT_B + 640, p0, p1, o);
      phase<false>(sb, sa, k01, nullptr, qe, kf, kp + G + GRP_B + 1024, qf, v0, vp + G + GRP_B, v1, vp + G + 3 * VT_B + 640, p0, p1, o);
      asm volatile("" : "+v"(o), "+v"(sa), "+v"(p1));
      if constexpr ((g & 3) == 3) {
        kq += 4 * GRP_B; krq += 4 * GRP_B; vq += 4 * GRP_B;
        asm volatile("" : "+v"(kq), "+v"(krq), "+v"(vq));
        kp = smem + kq; krp = smem + krq; vp = smem + vq;
      }
    };
#define GR(i) group(IC<i>{});
    GR(0) GR(1) GR(2) GR(3) GR(4) GR(5) GR(6) GR(7) GR(8) GR(9) GR(10) GR(11) GR(12) GR(13) GR(14) GR(15)
    o = MF16(v1, __builtin_bit_cast(bf16x8, p1), o);
  }
  STAMP(4); CSTAMP(11); LSTAMP(32);
  lds_add(ctl + C_DONE);
  float l = h ? o[12] : o[10];
  float big = l;
#pragma unroll
  for (int i = 0; i < 10; ++i) big = fmaxf(big, fabsf(o[i]));
  bool ok = (big < 1.0e37f) && (l > 0.f);
#pragma unroll
  for (int i = 0; i < 10; ++i) ok = ok && (o[i] == o[i]);
  if (!__all(ok)) {
    attn_exact(ws + WS_KV, ko, kro, vo, qf, qrem, h, o);
    l = h ? o[12] : o[10];
  }
  {
    const float inv = __builtin_amdgcn_rcpf(l);
    unsigned* dst = (unsigned*)(smem + L_OB + wave * ROWB + c32 * 36 + h * 8);
    dst[0] = pk2(o[0] * inv, o[1] * inv);
    dst[1] = pk2(o[2] * inv, o[3] * inv);
    dst[4] = pk2(o[4] * inv, o[5] * inv);
    dst[5] = pk2(o[6] * inv, o[7] * inv);
    if (!h) dst[8] = pk2(o[8] * inv, o[9] * inv);
    store_row(smem + L_OB + wave * ROWB, ws + WS_OB + (size_t)row * ROWB, lane);
  }
  char* cntB = ws + WS_CNT + (128 + (row >> 4)) * 128;
  if (!has_tile) { signal_row(cntB, lane); return; }
  const int xa = fr * 128 + ((((wave >> 2) * 4 + kg) ^ (fr & 7)) << 4);
  char* pw = smem + L_P + (wave >> 2) * P_HALF + fr * P_LD + (wave & 3) * 64 + kg * 16;
  f32x4 acc[5];
  auto mm3 = [&](u32x4 (&w)[9], int c0) {
#pragma unroll
    for (int c = c0; c < c0 + 3; ++c) {
      bf16x8 xf[5];
#pragma unroll
      for (int m = 0; m < 5; ++m) xf[m] = *(const bf16x8*)(smem + c * XC_B + xa + m * 2048);
#pragma unroll
      for (int m = 0; m < 5; ++m) acc[m] = MFMA16(__builtin_bit_cast(bf16x8, w[c]), xf[m], c ? acc[m] : f32x4{});
    }
  };
  auto put = [&] {
#pragma unroll
    for (int m = 0; m < 5; ++m) *(f32x4*)(pw + m * 16 * P_LD) = acc[m];
  };
  auto tile_out = [&](int br) {
#pragma unroll
    for (int i = 0; i < 2; ++i) {
      const int r = (tid >> 3) + 64 * i, c = tid & 7;
      if (r < nrt * 16) {
        const char* p = smem + L_P + r * P_LD + c * 32;
        const f32x4 a0 = *(const f32x4*)p, a1 = *(const f32x4*)(p + 16);
        const f32x4 b0 = *(const f32x4*)(p + P_HALF), b1 = *(const f32x4*)(p + P_HALF + 16);
        const u32x4 u = {pk2(a0[0] + b0[0], a0[1] + b0[1]), pk2(a0[2] + b0[2], a0[3] + b0[3]),
                         pk2(a1[0] + b1[0], a1[1] + b1[1]), pk2(a1[2] + b1[2], a1[3] + b1[3])};
        ST16(out + (size_t)(rt0 * 16 + r) * (2 * ROWB) + br * ROWB + cg * 128 + c * 16, u);
      }
    }
  };
  signal_row(cntB, lane);
  STAMP(5); LSTAMP(33);
  lds_wait(ctl + C_XA, 8);
  mm3(wA, 0);
  mm3(wA, 3);
  mm3(wA, 6);
  LSTAMP(34);
  lds_wait(ctl + C_DONE, 8);
  put();
  LSTAMP(35);
  if (wave == 0) cnt_wait(cnt_p + 128 * 128);
  bar_lgkm();
  STAMP(6); LSTAMP(36);
  fetch_tile(ws + WS_OB);
  LSTAMP(37);
  tile_out(0);
  LSTAMP(38);
  if (nrt == 5) wait_vm<8>(); else wait_vm<6>();
  bar_lgkm();
  STAMP(7); LSTAMP(39);
  mm3(wB, 0);
  if (nrt == 5) wait_vm<4>(); else wait_vm<3>();
  bar_lgkm();
  mm3(wB, 3);
  wait_vm<0>();
  bar_lgkm();
  mm3(wB, 6);
  put();
  bar_lgkm();
  STAMP(8);
  tile_out(1);
  STAMP(9);
  STAMP_OUT(1, ws); LSTAMP_OUT(ws);
}

extern "C" void kernel_launch(void* const* d_in, const int* in_sizes, int n_in, void* d_out, int out_size,
                              void* d_ws, size_t ws_size, hipStream_t stream) {
  (void)in_sizes; (void)n_in; (void)out_size; (void)ws_size;
  (void)hipFuncSetAttribute((const void*)k1_proj, hipFuncAttributeMaxDynamicSharedMemorySize, K1_LDS);
  (void)hipFuncSetAttribute((const void*)k2_attn, hipFuncAttributeMaxDynamicSharedMemorySize, K2_LDS_ALL);
  char* ws = (char*)d_ws;
  k1_proj<<<256, 512, K1_LDS, stream>>>((const char*)d_in[0], (const char*)d_in[1], (const char*)d_in[3], (const char*)d_in[4],
                                        (const char*)d_in[5], (const char*)d_in[6], (const char*)d_in[2], (const char*)d_in[7], ws);
  k2_attn<<<256, 512, K2_LDS_ALL, stream>>>(ws, (char*)d_out);
}
```

```cpp
#include <hip/hip_runtime.h>
#include <hip/hip_bf16.h>
#include <stdint.h>

typedef __bf16 bf16x2 __attribute__((ext_vector_type(2)));
typedef __bf16 bf16x8 __attribute__((ext_vector_type(8)));
typedef short s16x4 __attribute__((ext_vector_type(4)));
typedef float f32x2 __attribute__((ext_vector_type(2)));
typedef float f32x4 __attribute__((ext_vector_type(4)));
typedef float f32x16 __attribute__((ext_vector_type(16)));
typedef unsigned u32x2 __attribute__((ext_vector_type(2)));
typedef unsigned u32x4 __attribute__((ext_vector_type(4)));
#define LDSP(T) __attribute__((address_space(3))) T*
#define DI __device__ __forceinline__
#define STAMP_DECL
#define STAMP(i) do {} while (0)
#define CSTAMP(i) do {} while (0)
#define LSTAMP(i) do {} while (0)
#define LSTAMP_OUT(w) do {} while (0)
#define STAMP_ARG
#define STAMP_PASS
#define STAMP_OUT(kid, wsp) do {} while (0)

#define K1UNROLL _Pragma("unroll")
#define PRIO(g) ((g) < 0 ? -1 : 3 - (((g) >> 1) & 3))
#define POLL_SLEEP "4"
#define SIGC 3
constexpr int SEQ = 2048, DM = 576;
constexpr int ROWB = DM * 2;
constexpr float LOG2E = 1.4426950408889634f;
constexpr float QSCALE = 0.23570226039551584f * LOG2E;

constexpr int GRP_B = 9728, KR_OFF = 4096, V_OFF = 4608, VT_B = 1280;
constexpr int KV_BYTES = 16 * GRP_B;
constexpr size_t WS_KV = 0;
constexpr size_t WS_QB = WS_KV + KV_BYTES;
constexpr size_t WS_QKV = WS_QB + (size_t)SEQ * ROWB;
constexpr size_t WS_OA = WS_QKV + (size_t)SEQ * 3 * ROWB;
constexpr size_t WS_OB = WS_OA + (size_t)SEQ * ROWB;
constexpr size_t WS_WF = WS_OB + (size_t)SEQ * ROWB;
constexpr size_t WS_F = WS_WF + (size_t)72 * 18432;
constexpr int F_OA = 0, F_KV = 2048, F_Q = F_KV + 64, F_EPOCH = F_Q + 64 * 16, F_ACK = F_EPOCH + 16, F_QKV = F_ACK + 256,
              F_WF = F_QKV + 32 * 32, F_OB = F_WF + 128;
typedef unsigned long long u64;

template <int N> struct IC { static constexpr int value = N; };
DI unsigned pk2(float a, float b) {
  f32x2 f = {a, b};
  return __builtin_bit_cast(unsigned, __builtin_convertvector(f, bf16x2));
}
DI float dot2(unsigned a, unsigned b, float c) {
  return __builtin_amdgcn_fdot2_f32_bf16(__builtin_bit_cast(bf16x2, a), __builtin_bit_cast(bf16x2, b), c, false);
}
DI float bf_lo(unsigned u) { return __uint_as_float(u << 16); }
DI float bf_hi(unsigned u) { return __uint_as_float(u & 0xffff0000u); }

DI void glds16(unsigned voff, const void* sbase, unsigned lds_dst) {
  unsigned keep;
  asm volatile("s_mov_b32 %0, m0\n\ts_mov_b32 m0, %3\n\ts_nop 0\n\tglobal_load_lds_dwordx4 %1, %2\n\ts_mov_b32 m0, %0"
               : "=&s"(keep) : "v"(voff), "s"(sbase), "s"(lds_dst) : "memory");
}
DI void glds4(unsigned voff, const void* sbase, unsigned lds_dst) {
  unsigned keep;
  asm volatile("s_mov_b32 %0, m0\n\ts_mov_b32 m0, %3\n\ts_nop 0\n\tglobal_load_lds_dword %1, %2\n\ts_mov_b32 m0, %0"
               : "=&s"(keep) : "v"(voff), "s"(sbase), "s"(lds_dst) : "memory");
}
template <int N> DI void wait_vm() { asm volatile("s_waitcnt vmcnt(%0)" ::"i"(N) : "memory"); }
DI void bar_lgkm() { asm volatile("s_waitcnt lgkmcnt(0)\n\ts_barrier" ::: "memory"); }
DI bf16x8 tr_frag(const char* p) {
  s16x4 lo = __builtin_amdgcn_ds_read_tr16_b64_v4i16((LDSP(s16x4))(p));
  s16x4 hi = __builtin_amdgcn_ds_read_tr16_b64_v4i16((LDSP(s16x4))(p + 256));
  return __builtin_bit_cast(bf16x8, __builtin_shufflevector(lo, hi, 0, 1, 2, 3, 4, 5, 6, 7));
}
#define ST16(p, v) __builtin_nontemporal_store(v, (u32x4*)(p))
#define MFMA16(a, b, c) __builtin_amdgcn_mfma_f32_16x16x32_bf16(a, b, c, 0, 0, 0)

constexpr int K2_LDS = KV_BYTES + 6144;
constexpr float M_OFF = 96.0f;
#define SB __builtin_amdgcn_sched_barrier(0)
#define MF16(a, b, c) __builtin_amdgcn_mfma_f32_32x32x16_bf16(a, b, c, 0, 0, 0)
#define MF8(a, b, c) __builtin_amdgcn_mfma_f32_32x32x8bf16_1k(a, b, c, 0, 0, 0)
#define EX(i) cur[i] = __builtin_amdgcn_exp2f(cur[i])

DI float xhalf(float x);
DI float max16(const f32x16& s, float m) {
#pragma unroll
  for (int i = 0; i < 16; i += 2) m = __builtin_fmaxf(__builtin_fmaxf(s[i], s[i + 1]), m);
  return m;
}
#define E(i) EX(i); SB;
#define C0(i) p0[i] = pk2(cur[2 * i], cur[2 * i + 1]); SB;
#define C1(i) p1[i] = pk2(cur[8 + 2 * i], cur[9 + 2 * i]); SB;
template <bool RELOAD>
DI void phase(f32x16& cur, f32x16& nxt, s16x4& kra, const char* krp, const s16x4 qr, bf16x8& kf, const char* kp2, const bf16x8 qf,
              bf16x8& v0, const char* v0p, bf16x8& v1, const char* v1p, u32x4& p0, u32x4& p1, f32x16& o) {
  const f32x16 z = {};
  nxt = MF8(kra, qr, z); SB;
  if (RELOAD) { kra = *(const s16x4*)krp; SB; }
  E(0) E(1) E(2) E(3) C0(0) E(4)
  o = MF16(v1, __builtin_bit_cast(bf16x8, p1), o); SB;
  v1 = *(const bf16x8*)v1p; SB;
  C0(1) E(5) E(6) E(7) C0(2) E(8)
  nxt = MF16(kf, qf, nxt); SB;
  kf = *(const bf16x8*)kp2; SB;
  C0(3) E(9) E(10) E(11) C1(0) E(12)
  o = MF16(v0, __builtin_bit_cast(bf16x8, p0), o); SB;
  v0 = *(const bf16x8*)v0p; SB;
  C1(1) E(13) E(14) E(15) C1(2) C1(3)
}
DI unsigned rem_word(unsigned qrem, float m, int h) {
  const unsigned hi = pk2(-m, 0.f) & 0xffffu;
  const unsigned lo = pk2(-m - bf_lo(hi), 0.f) & 0xffffu;
  return h ? (hi | (lo << 16)) : qrem;
}
DI s16x4 mk2(unsigned a, unsigned b) { u32x2 r = {a, b}; return __builtin_bit_cast(s16x4, r); }

DI float max_pass(const char* kp, const char* krp, const bf16x8 qf, unsigned rw, int npair) {
  const s16x4 qe = mk2(rw, 0u), qo = mk2(0u, rw);
  const f32x16 z = {};
  float m = -3.0e38f;
  for (int i = 0; i < npair; ++i) {
    const int G = (i >> 1) * GRP_B, hf = i & 1;
    const s16x4 kr = *(const s16x4*)(krp + G + hf * 8);
    m = max16(MF16(*(const bf16x8*)(kp + G + hf * 2048), qf, MF8(kr, qe, z)), m);
    m = max16(MF16(*(const bf16x8*)(kp + G + hf * 2048 + 1024), qf, MF8(kr, qo, z)), m);
  }
  return fmaxf(m, xhalf(m));
}
DI void attn_exact(const char* img, int ko, int kro, int vo, const bf16x8 qf, unsigned qrem, int h, f32x16& o) {
  const float mt = max_pass(img + ko, img + kro, qf, rem_word(qrem, 0.f, h), 32);
  const unsigned rw = rem_word(qrem, mt, h);
  const s16x4 qe = mk2(rw, 0u), qo = mk2(0u, rw);
  const f32x16 z = {};
  o = z;
  for (int t = 0; t < 64; ++t) {
    const char* g = img + (t >> 2) * GRP_B;
    const s16x4 kr = *(const s16x4*)(g + kro + (t & 2) * 4);
    f32x16 cur = MF16(*(const bf16x8*)(g + ko + (t & 3) * 1024), qf, MF8(kr, (t & 1) ? qo : qe, z));
    u32x4 p0, p1;
#pragma unroll
    for (int i = 0; i < 16; ++i) EX(i);
#pragma unroll
    for (int i = 0; i < 4; ++i) { p0[i] = pk2(cur[2 * i], cur[2 * i + 1]); p1[i] = pk2(cur[8 + 2 * i], cur[9 + 2 * i]); }
    o = MF16(*(const bf16x8*)(g + vo + (t & 3) * VT_B), __builtin_bit_cast(bf16x8, p0), o);
    o = MF16(*(const bf16x8*)(g + vo + (t & 3) * VT_B + 640), __builtin_bit_cast(bf16x8, p1), o);
  }
}

constexpr int LA_0 = 98304;
constexpr int LA_Q = LA_0 + 22 * 2304;
constexpr int XC_B = 10240;
constexpr int L_OB = 92160;
constexpr int L_XB = 102400;
constexpr int P_LD = 272, P_HALF = 80 * P_LD;
constexpr int L_TB = 2 * P_HALF, TB_LD = 272;
constexpr int L_EB = L_TB + 32 * TB_LD;
constexpr int L_CTL = K2_LDS;
constexpr int K2_LDS_ALL = K2_LDS + 64;
enum { C_KVA = 0, C_KVB = 4, C_PAST = 8, C_AROWS = 12, C_A2 = 16, C_XA = 20, C_KV0 = 24, C_GO = 28, C_GO1 = 32, C_STG = 36, C_TOK = 40 };

DI void lds_add(unsigned addr) {
  asm volatile("s_waitcnt lgkmcnt(0)\n\ts_mov_b64 exec, 1\n\tds_add_u32 %0, %1\n\ts_mov_b64 exec, -1" ::"v"(addr), "v"(1u) : "memory");
}
template <int N> DI void lds_wait(unsigned addr) {
  unsigned r, t;
  asm volatile("1:\n\tds_read_b32 %0, %2\n\ts_waitcnt lgkmcnt(0)\n\tv_readfirstlane_b32 %1, %0\n\ts_cmp_lt_u32 %1, %3\n\ts_cbranch_scc1 1b"
               : "=&v"(r), "=&s"(t) : "v"(addr), "i"(N) : "memory", "scc");
}
DI float xhalf(float x) {
  const unsigned u = __float_as_uint(x);
  const auto r = __builtin_amdgcn_permlane32_swap(u, u, false, false);
  return __uint_as_float((threadIdx.x & 32) ? r[0] : r[1]);
}
DI void local_attn(char* smem, unsigned lds0, int wave, int c32, int h, int row) {
  unsigned* qp = (unsigned*)(smem + LA_Q + wave * ROWB + c32 * 36);
  unsigned q[9];
#pragma unroll
  for (int e = 0; e < 9; ++e) q[e] = qp[e];
  const unsigned kb = lds0 + LA_0 + (wave + 8 - h * 8) * 2304 + c32 * 36;
  const unsigned vb = lds0 + LA_0 + wave * 2304 + ROWB + c32 * 36 + h * 16;
  u32x2 kk[9][2], vv[5][4];
#pragma unroll
  for (int e = 0; e < 9; ++e)
    asm volatile("ds_read2st64_b32 %0, %2 offset0:54 offset1:36\n\tds_read2st64_b32 %1, %2 offset0:18 offset1:0"
                 : "=&v"(kk[e][0]), "=&v"(kk[e][1]) : "v"(kb + e * 4) : "memory");
  asm volatile("s_waitcnt lgkmcnt(0)" : "+v"(kk[0][0]), "+v"(kk[0][1]), "+v"(kk[1][0]), "+v"(kk[1][1]), "+v"(kk[2][0]), "+v"(kk[2][1]),
      "+v"(kk[3][0]), "+v"(kk[3][1]), "+v"(kk[4][0]), "+v"(kk[4][1]), "+v"(kk[5][0]), "+v"(kk[5][1]), "+v"(kk[6][0]), "+v"(kk[6][1]),
      "+v"(kk[7][0]), "+v"(kk[7][1]), "+v"(kk[8][0]), "+v"(kk[8][1])::"memory");
#pragma unroll
  for (int e = 0; e < 5; ++e)
    asm volatile("ds_read2st64_b32 %0, %4 offset0:126 offset1:108\n\tds_read2st64_b32 %1, %4 offset0:90 offset1:72\n\t"
                 "ds_read2st64_b32 %2, %4 offset0:54 offset1:36\n\tds_read2st64_b32 %3, %4 offset0:18 offset1:0"
                 : "=&v"(vv[e][0]), "=&v"(vv[e][1]), "=&v"(vv[e][2]), "=&v"(vv[e][3]) : "v"(vb + e * 4) : "memory");
  float lg[4] = {0.f, 0.f, 0.f, 0.f};
#pragma unroll
  for (int e = 0; e < 9; ++e) {
    const unsigned k0 = kk[e][0][0], k1 = kk[e][0][1], k2 = kk[e][1][0], k3 = kk[e][1][1];
    lg[0] = dot2(q[e], k0, lg[0]); lg[1] = dot2(q[e], k1, lg[1]); lg[2] = dot2(q[e], k2, lg[2]); lg[3] = dot2(q[e], k3, lg[3]);
  }
#pragma unroll
  for (int w = 0; w < 4; ++w) lg[w] = row - 2 * (h * 4 + w) >= 0 ? lg[w] * QSCALE : -1.0e9f * LOG2E;
  float mx = fmaxf(fmaxf(lg[0], lg[1]), fmaxf(lg[2], lg[3]));
  mx = fmaxf(mx, xhalf(mx));
  float sum = 0.f;
#pragma unroll
  for (int w = 0; w < 4; ++w) { lg[w] = __builtin_amdgcn_exp2f(lg[w] - mx); sum += lg[w]; }
  sum += xhalf(sum);
  const float inv = __builtin_amdgcn_rcpf(sum);
  const unsigned pa = pk2(lg[0] * inv, lg[1] * inv), pb = pk2(lg[2] * inv, lg[3] * inv);
  const auto ra = __builtin_amdgcn_permlane32_swap(pa, pa, false, false);
  const auto rb = __builtin_amdgcn_permlane32_swap(pb, pb, false, false);
  const unsigned pp[4] = {ra[0], rb[0], ra[1], rb[1]};
  asm volatile("s_waitcnt lgkmcnt(0)" : "+v"(vv[0][0]), "+v"(vv[0][1]), "+v"(vv[0][2]), "+v"(vv[0][3]), "+v"(vv[1][0]), "+v"(vv[1][1]),
      "+v"(vv[1][2]), "+v"(vv[1][3]), "+v"(vv[2][0]), "+v"(vv[2][1]), "+v"(vv[2][2]), "+v"(vv[2][3]), "+v"(vv[3][0]), "+v"(vv[3][1]),
      "+v"(vv[3][2]), "+v"(vv[3][3]), "+v"(vv[4][0]), "+v"(vv[4][1]), "+v"(vv[4][2]), "+v"(vv[4][3])::"memory");
#pragma unroll
  for (int e = 0; e < 5; ++e) {
    float o0 = 0.f, o1 = 0.f;
#pragma unroll
    for (int w2 = 0; w2 < 4; ++w2) {
      const unsigned x = vv[e][w2][0], y = vv[e][w2][1];
      o0 = dot2(__builtin_amdgcn_perm(y, x, 0x05040100u), pp[w2], o0);
      o1 = dot2(__builtin_amdgcn_perm(y, x, 0x07060302u), pp[w2], o1);
    }
    qp[h * 4 + e] = pk2(o0, o1);
  }
}
template <int MASK> DI void glds16_part(unsigned voff, const void* sbase, unsigned lds_dst) {
  unsigned keep;
  asm volatile("s_mov_b32 %0, m0\n\ts_mov_b32 m0, %3\n\ts_mov_b64 exec, %4\n\tglobal_load_lds_dwordx4 %1, %2 sc1\n\ts_mov_b64 exec, -1\n\ts_mov_b32 m0, %0"
               : "=&s"(keep) : "v"(voff), "s"(sbase), "s"(lds_dst), "i"(MASK) : "memory");
}
DI void glds16_sc1(unsigned voff, const void* sbase, unsigned lds_dst) {
  unsigned keep;
  asm volatile("s_mov_b32 %0, m0\n\ts_mov_b32 m0, %3\n\ts_nop 0\n\tglobal_load_lds_dwordx4 %1, %2 sc1\n\ts_mov_b32 m0, %0"
               : "=&s"(keep) : "v"(voff), "s"(sbase), "s"(lds_dst) : "memory");
}
DI void store_row(const char* stage, char* dst, int lane) {
  asm volatile("s_waitcnt lgkmcnt(0)" ::: "memory");
  const u32x4 a = *(const u32x4*)(stage + lane * 16);
  const u32x4 b = *(const u32x4*)(stage + 1024 + (lane & 7) * 16);
  asm volatile("global_store_dwordx4 %0, %1, off sc1\n\ts_nop 1" ::"v"(dst + lane * 16), "v"(a) : "memory");
  if (lane < 8) asm volatile("global_store_dwordx4 %0, %1, off offset:1024 sc1\n\ts_nop 1" ::"v"(dst + lane * 16), "v"(b) : "memory");
}
DI void st16_sc1(char* p, u32x4 v) { asm volatile("global_store_dwordx4 %0, %1, off sc1\n\ts_nop 1" ::"v"(p), "v"(v) : "memory"); }
DI void st4_sc1(char* p, unsigned v) { asm volatile("global_store_dword %0, %1, off sc1" ::"v"(p), "v"(v) : "memory"); }
DI void signal(u64* f, u64 tok) {
  asm volatile("s_waitcnt vmcnt(0)\n\ts_mov_b64 exec, 1\n\tglobal_store_dwordx2 %0, %1, off sc1\n\ts_mov_b64 exec, -1" ::"v"(f), "v"(tok) : "memory");
}
DI void poll(const u64* p, u64 tok) {
  u64 r; unsigned n;
  asm volatile("s_mov_b32 %1, 0x200000\n1:\n\tglobal_load_dwordx2 %0, %2, off sc1\n\ts_sub_u32 %1, %1, 1\n\ts_cbranch_scc1 2f\n\t"
               "s_waitcnt vmcnt(0)\n\tv_cmp_ne_u64 vcc, %3, %0\n\ts_cbranch_vccz 2f\n\ts_sleep " POLL_SLEEP "\n\ts_branch 1b\n2:\n\ts_waitcnt vmcnt(0)"
               : "=&v"(r), "=&s"(n) : "v"(p), "s"(tok) : "memory", "scc", "vcc");
}
#define POLL_ISSUE(r, p) asm volatile("global_load_dwordx2 %0, %1, off sc1" : "=v"(r) : "v"(p) : "memory")
#define POLL_CHECK(r, p, tok) do { asm volatile("s_waitcnt vmcnt(0)" : "+v"(r)::"memory"); if (!__all((r) == (tok))) poll(p, tok); } while (0)

constexpr int G_XB = 16384, G_STAGE = 32768, G_RING = 30720;
constexpr int L_S1 = 0, L_S2 = LA_0, ST_LD = 144;

template <int KIND> struct Loader {
  static constexpr int NX = KIND == 2 ? 4 : (KIND == 3 ? 0 : 2), P = NX + (KIND == 1 ? 10 : 4);
  unsigned xo[4], wo[4], eo[4];
  const char *X, *W0, *W1, *wk, *wv;
  int ldw, ks, l;
  unsigned lds0;
  DI void init(const char* X_, int row0, const char* W0_, const char* W1_, int ldw_, int cb0, int cb1, const char* wk_, const char* wv_,
               int ks0, unsigned lds0_, int l_, int lane) {
    X = X_; W0 = W0_; W1 = W1_; wk = wk_; wv = wv_; ldw = ldw_; ks = ks0; lds0 = lds0_; l = l_;
#pragma unroll
    for (int i = 0; i < 4; ++i) {
      xo[i] = (row0 + (l + 4 * i) * 8 + (lane >> 3)) * ROWB + (((lane & 7) ^ (lane >> 3)) << 4);
      const int kp = l + 4 * (i >> 1);
      wo[i] = (kp * 8 + ((lane >> 2) & 7)) * ldw + ((i & 1) ? cb1 : cb0) + (lane >> 5) * 64 + (((lane & 3) ^ ((kp & 1) << 1)) << 4);
      int dw = (lane & 15) ^ ((kp & 1) << 3);
      dw = dw > 8 ? 8 : dw;
      eo[i] = (kp * 8 + (i & 1) * 4 + (lane >> 4)) * 36 + dw * 4;
    }
  }
  DI void issue(int t) {
    const unsigned sb = lds0 + (t & 3) * G_STAGE;
#pragma unroll
    for (int i = 0; i < NX; ++i) glds16(xo[i], X + ks * 128, sb + (l + 4 * i) * 1024);
    const char* w0 = W0 + (size_t)ks * 64 * ldw;
    glds16(wo[0], w0, sb + G_XB + l * 2048);
    glds16(wo[2], w0, sb + G_XB + (l + 4) * 2048);
    if (KIND != 1) {
      const char* w1 = W1 + (size_t)ks * 64 * ldw;
      glds16(wo[1], w1, sb + G_XB + l * 2048 + 1024);
      glds16(wo[3], w1, sb + G_XB + (l + 4) * 2048 + 1024);
    } else {
#pragma unroll
      for (int i = 0; i < 4; ++i) {
        const unsigned d = sb + G_XB + (l + 4 * (i >> 1)) * 2048 + 1024 + (i & 1) * 256;
        glds4(eo[i], wk + ks * (64 * 36), d);
        glds4(eo[i], wv + ks * (64 * 36), d + 512);
      }
    }
    ks = ks == 8 ? 0 : ks + 1;
  }
  DI void first() {
    issue(0); issue(1);
    wait_vm<P>(); bar_lgkm();
    issue(2);
#pragma unroll 1
    for (int t = 3; t < 9; ++t) { issue(t); wait_vm<2 * P>(); bar_lgkm(); }
  }
  template <int PPREV, int N> DI void second() {
    issue(9); wait_vm<P + PPREV>(); bar_lgkm();
#pragma unroll 1
    for (int t = 10; t < 9 + N; ++t) { issue(t); wait_vm<2 * P>(); bar_lgkm(); }
  }
  template <class H1, class H2> DI void last(H1 h1, H2 h2) { wait_vm<P>(); bar_lgkm(); h1(); wait_vm<0>(); bar_lgkm(); h2(); bar_lgkm(); }
};

template <int MT, class HOOK>
DI void gemm_job(const char* smem, int s0, int wm, int trE, int trO, int fr, int kg, f32x4 (&acc)[MT][4], HOOK hook) {
  const int xo0 = (wm * MT * 16 + fr) * 128 + ((kg ^ (fr & 7)) << 4), xo1 = xo0 ^ 64;
  bf16x8 xa[MT], wa[4], xb[MT], wb[4];
  auto rd = [&](const char* sb, int kk, bf16x8 (&xf)[MT], bf16x8 (&wf)[4]) {
#pragma unroll
    for (int m = 0; m < MT; ++m) xf[m] = *(const bf16x8*)(sb + (kk ? xo1 : xo0) + m * 2048);
#pragma unroll
    for (int n = 0; n < 4; ++n) wf[n] = tr_frag(sb + ((n & 1) ? trO : trE) + kk * 8192 + (n >> 1) * 512);
  };
  auto mm = [&](bf16x8 (&xf)[MT], bf16x8 (&wf)[4]) {
#pragma unroll
    for (int n = 0; n < 4; ++n)
#pragma unroll
      for (int m = 0; m < MT; ++m) acc[m][n] = MFMA16(wf[n], xf[m], acc[m][n]);
  };
  auto ilv = [&] {
    constexpr int NR = MT + 8, NM = 4 * MT, RPM = NM >= NR ? 1 : 2;
#pragma unroll
    for (int q = 0; q < (NR + RPM - 1) / RPM; ++q) { __builtin_amdgcn_sched_group_barrier(0x8, 1, 0); __builtin_amdgcn_sched_group_barrier(0x100, RPM, 0); }
    __builtin_amdgcn_sched_group_barrier(0x8, NM, 0);
    __builtin_amdgcn_sched_barrier(0);
  };
  rd(smem + (s0 & 3) * G_STAGE, 0, xa, wa);
#pragma unroll
  for (int j = 0; j < 9; ++j) {
    rd(smem + ((s0 + j) & 3) * G_STAGE, 1, xb, wb);
    mm(xa, wa);
    ilv();
    bar_lgkm();
    if (j == 1) hook();
    if (j < 8) rd(smem + ((s0 + j + 1) & 3) * G_STAGE, 0, xa, wa);
    mm(xb, wb);
    ilv();
  }
}
template <int MT>
DI void stage_tile(char* st, f32x4 (&acc)[MT][4], bool scale, int fr, int kg) {
#pragma unroll
  for (int m = 0; m < MT; ++m)
#pragma unroll
    for (int n = 0; n < 4; ++n) {
      u32x2 u = {pk2(acc[m][n][0], acc[m][n][1]), pk2(acc[m][n][2], acc[m][n][3])};
      if (scale) {
        u[0] = pk2(bf_lo(u[0]) * QSCALE, bf_hi(u[0]) * QSCALE);
        u[1] = pk2(bf_lo(u[1]) * QSCALE, bf_hi(u[1]) * QSCALE);
      }
      *(u32x2*)(st + (m * 16 + fr) * ST_LD + n * 32 + kg * 8) = u;
    }
  asm volatile("s_waitcnt lgkmcnt(0)" ::: "memory");
}
template <int MT>
DI void store_tile(const char* st, char* dst, int ld, int lane) {
  u32x4 v[MT * 2];
#pragma unroll
  for (int i = 0; i < MT * 2; ++i) v[i] = *(const u32x4*)(st + (i * 8 + (lane >> 3)) * ST_LD + (lane & 7) * 16);
  char* d = dst + (size_t)(lane >> 3) * ld + (lane & 7) * 16;
#pragma unroll
  for (int i = 0; i < MT * 2; ++i) st16_sc1(d + (size_t)i * 8 * ld, v[i]);
}

__global__ __launch_bounds__(512) void k_all(const char* __restrict__ xA, const char* __restrict__ wqkv, const char* __restrict__ woA,
                                             const char* __restrict__ xB, const char* __restrict__ wq, const char* __restrict__ wk,
                                             const char* __restrict__ wv, const char* __restrict__ woB,
                                             char* __restrict__ ws, char* __restrict__ out) {
  extern __shared__ __attribute__((aligned(16))) char smem[];
  const int tid = threadIdx.x, lane = tid & 63;
  const int wave = __builtin_amdgcn_readfirstlane(tid >> 6);
  STAMP_DECL; STAMP(0);
  const int b = blockIdx.x;
  const int xg = b & 7, xi = b >> 3;
  const int rblk = xg * 32 + xi;
  const int row = rblk * 8 + wave;
  const int c32 = lane & 31, h = lane >> 5;
  const int fr = lane & 15, kg = lane >> 4;
  const unsigned lds0 = (unsigned)(uintptr_t)smem;
  const unsigned ctl = lds0 + L_CTL;
  const int ko = lane * 16;
  u64* const flg = (u64*)(ws + WS_F);
  u64 tok;
  bool pre = false;
  auto kv_fetch = [&](int p0, int step, int n) {
#pragma unroll
    for (int i = 0; i < n; ++i) glds16_sc1(ko, ws + WS_KV + (p0 + step * i) * 1024, lds0 + (p0 + step * i) * 1024);
  };
  {
    const bool has1 = xi < 20;
    const int rb1 = xg * 4 + (xi & 3), ct1 = 4 - (xi >> 2);
    const bool has2 = xi >= 4;
    const int rb2 = (xg >> 1) * 4 + (xi & 3), ct2 = (xg & 1) * 7 + 7 - (xi >> 2);
    const int ru = xi * 8 + xg;
    const bool hasr = xi < 4 && ru < 27;
    const int rl = ru / 3, rk = (ru - rl * 3) * 3;
    const int rot = rblk % 9;
    if (wave >= 4) {
      const int l = wave - 4;
      u64 pe0, pe1;
      auto early1 = [&] {
        POLL_ISSUE(pe0, flg + F_KV + lane);
        POLL_ISSUE(pe1, flg + F_Q + (row >> 5) * 16 + (lane < 9 ? lane : 0));
      };
      auto early2 = [&] {
        asm volatile("" : "+v"(pe0), "+v"(pe1));
        const u64 tk = *(const u64*)(smem + L_CTL + C_TOK);
        if (__all(pe0 == tk && pe1 == tk)) { pre = true; lds_add(ctl + C_GO1); kv_fetch(l, 4, 15); }
      };
      Loader<2> g2;
      if (has2) g2.init(xA, rb2 * 128, wqkv, wqkv, 3 * ROWB, ct2 * 256, ct2 * 256 + (ct2 == 13 ? 0 : 128), nullptr, nullptr, rot, lds0 + G_RING, l, lane);
      if (!has1) { g2.first(); g2.last([] {}, [] {}); }
      else if (has2) {
        Loader<0> g1;
        g1.init(xB, rb1 * 64, wq, wq, ROWB, ct1 * 256, ct1 * 256 + 128, nullptr, nullptr, rot, lds0 + G_RING, l, lane);
        g1.first();
        g2.second<6, 9>();
        g2.last(early1, early2);
      } else {
        Loader<1> g1;
        g1.init(xB, rb1 * 64, wq, wq, ROWB, 1024, 0, wk, wv, rot, lds0 + G_RING, l, lane);
        g1.first();
        if (hasr) {
          const int c0 = rl * 128, c1 = c0 + 64;
          Loader<3> g3;
          g3.init(nullptr, 0, c0 < DM ? woA : woB, c1 < DM ? woA : woB, ROWB, (c0 < DM ? c0 : c0 - DM) * 2, (c1 < DM ? c1 : c1 - DM) * 2,
                  nullptr, nullptr, rk, lds0 + G_RING, l, lane);
          g3.second<12, 3>();
          g3.last([] {}, [] {});
        } else g1.last([] {}, [] {});
      }
      tok = *(const u64*)(smem + L_CTL + C_TOK);
    } else {
      if (wave == 0) {
        u64 e;
        asm volatile("global_load_dwordx2 %0, %1, off sc1\n\ts_waitcnt vmcnt(0)" : "=&v"(e) : "v"(flg + F_EPOCH) : "memory");
        e += 0x9e3779b97f4a7c15ull;
        if (lane < 10) *(unsigned*)(smem + L_CTL + lane * 4) = 0u;
        if (lane == 0) *(u64*)(smem + L_CTL + C_TOK) = e;
        signal(flg + F_ACK + rblk, e);
      }
      if (wave == 1) {
        const unsigned scr = lds0 + 28672;
        const unsigned pw = (unsigned)(xi * 8 + (lane & 7)) * 65536u, po = (unsigned)(xi * 4 + (lane & 3)) * 65536u;
        if (lane < 8) glds4(pw < (unsigned)WS_F ? pw : (unsigned)WS_F, ws, scr);
        if (lane < 4) glds4(po < 4718592u ? po : 0u, out, scr);
      }
      const int wm = wave & 1, wn = wave >> 1;
      const int tro = G_XB + kg * 2048 + (fr >> 2) * 64 + (fr & 3) * 8;
      const int trE = tro + ((kg & 1) << 5), trO = tro + (((kg & 1) ^ 1) << 5);
      bar_lgkm();
      STAMP(1);
      tok = *(const u64*)(smem + L_CTL + C_TOK);
      u64* const fq1 = flg + F_Q + (rb1 * 2 + wm) * 16 + ct1 * 2 + wn;
      if (has1) {
        f32x4 acc[2][4] = {};
        gemm_job<2>(smem + G_RING, 0, wm, trE + wn * 1024, trO + wn * 1024, fr, kg, acc, [] { wait_vm<0>(); });
        char* st = smem + L_S1 + wave * (32 * ST_LD);
        const bool kvw = ct1 == 4 && wn == 1;
        stage_tile<2>(st, acc, !kvw, fr, kg);
        const int t = rb1 * 2 + wm;
        if (!kvw) {
          store_tile<2>(st, ws + WS_QB + (size_t)t * 32 * ROWB + ct1 * 256 + wn * 128, ROWB, lane);
          if (!has2) signal(fq1, tok);
        } else {
          char* g = ws + WS_KV + (t >> 2) * GRP_B;
          st16_sc1(g + (t & 3) * 1024 + ko, *(const u32x4*)(st + c32 * ST_LD + h * 16));
          if (!h) st4_sc1(g + KR_OFF + c32 * 16 + (t & 3) * 4, *(const unsigned*)(st + c32 * ST_LD + 32));
#pragma unroll
          for (int i = 0; i < 2; ++i) {
            const int sl = lane + 64 * i;
            if (sl < 80) {
              const int blk = sl / 20, d = sl - blk * 20;
              u32x4 o;
              if (d < 18) {
                const char* src = st + ((blk >> 1) * 16 + (blk & 1) * 4) * ST_LD + 64 + d * 2;
#pragma unroll
                for (int e = 0; e < 4; ++e) {
                  const int r = (e >> 1) * 8 + (e & 1) * 2;
                  o[e] = (unsigned)*(const unsigned short*)(src + r * ST_LD) | ((unsigned)*(const unsigned short*)(src + (r + 1) * ST_LD) << 16);
                }
              } else {
                const unsigned f = d == 18 ? 0x3f803f80u : 0u;
                o = u32x4{f, f, f, f};
              }
              st16_sc1(g + V_OFF + (t & 3) * VT_B + sl * 16, o);
            }
          }
          signal(flg + F_KV + t, tok);
        }
      }
      STAMP(2);
      const int s0 = has1 ? 9 : 0;
      if (has2) {
        f32x4 acc[4][4] = {};
        gemm_job<4>(smem + G_RING, s0, wm, trE + wn * 1024, trO + wn * 1024, fr, kg, acc, [&] { wait_vm<0>(); if (has1) signal(fq1, tok); });
        STAMP(7);
        if (!(ct2 == 13 && wn == 1)) {
          char* st = smem + L_S2 + wave * (64 * ST_LD);
          stage_tile<4>(st, acc, false, fr, kg);
          const int t = rb2 * 2 + wm;
          store_tile<4>(st, ws + WS_QKV + (size_t)t * 64 * (3 * ROWB) + ct2 * 256 + wn * 128, 3 * ROWB, lane);
          signal(flg + F_QKV + t * 32 + ct2 * 2 + wn, tok);
        }
      } else if (hasr) {
        char* dst = ws + WS_WF + (size_t)(rl * 8 + wave * 2) * 18432 + rk * 2048 + ko;
#pragma unroll 1
        for (int j = 0; j < 3; ++j) {
          const char* sb = smem + G_RING + ((9 + j) & 3) * G_STAGE + wave * 512;
          const bf16x8 f0 = tr_frag(sb + trE), f1 = tr_frag(sb + trO), f2 = tr_frag(sb + trE + 8192), f3 = tr_frag(sb + trO + 8192);
          bar_lgkm();
          st16_sc1(dst + j * 2048, __builtin_bit_cast(u32x4, f0));
          st16_sc1(dst + j * 2048 + 18432, __builtin_bit_cast(u32x4, f1));
          st16_sc1(dst + j * 2048 + 1024, __builtin_bit_cast(u32x4, f2));
          st16_sc1(dst + j * 2048 + 18432 + 1024, __builtin_bit_cast(u32x4, f3));
        }
        signal(flg + F_WF + ru * 4 + wave, tok);
      }
      lds_add(ctl + C_STG);
    }
  }
  tok = ((u64)(unsigned)__builtin_amdgcn_readfirstlane((unsigned)(tok >> 32)) << 32) | (unsigned)__builtin_amdgcn_readfirstlane((unsigned)tok);
  bf16x8 qf;
  unsigned qrem;
  if (wave == 4 && !pre) {
    poll(flg + F_KV + lane, tok);
    poll(flg + F_Q + (row >> 5) * 16 + (lane < 9 ? lane : 0), tok);
    lds_add(ctl + C_GO1);
  }
  lds_wait<1>(ctl + C_GO1);
  if (wave >= 4 && !pre) {
    if (xi < 4) lds_wait<4>(ctl + C_STG);
    kv_fetch(wave - 4, 4, 15);
  }
  {
    const char* qp = ws + WS_QB + (size_t)row * ROWB + c32 * 36;
    asm volatile("global_load_dwordx4 %0, %1, off sc1" : "=v"(qf) : "v"(qp + h * 16) : "memory");
    asm volatile("global_load_dword %0, %1, off offset:32 sc1" : "=v"(qrem) : "v"(qp) : "memory");
  }
  if (wave >= 4) { wait_vm<9>(); lds_add(ctl + C_KV0); }
  const int vrow = c32 < 19 ? c32 : (c32 == 28 ? 18 : 19);
  const int kro = h ? V_OFF + 18 * 16 : KR_OFF + c32 * 16;
  const int vo = V_OFF + h * 320 + vrow * 16;
  const int rg = rblk / 9, cg = rblk - rg * 9;
  const int nrt = rg < 16 ? 5 : 4, rt0 = rg < 16 ? rg * 5 : 16 + rg * 4;
  const bool has_tile = rblk < 252;
  const unsigned xo = (lane >> 3) * ROWB + (((lane & 7) ^ (lane >> 3)) << 4);
  auto fetch_tile = [&](const char* src) {
    const char* s0 = src + (size_t)(rt0 * 16 + wave * 8) * ROWB;
    const char* s1 = src + (size_t)(rt0 * 16 + 64 + (wave & 1) * 8) * ROWB;
    if (nrt == 5) {
#pragma unroll
      for (int c = 0; c < 9; ++c) {
        glds16_sc1(xo, s0 + c * 128, lds0 + c * XC_B + wave * 1024);
        if (c % 3 == 2) {
          const int ce = c == 8 ? 8 : (c / 3) * 4 + (wave >> 1);
          glds16_sc1(xo, s1 + ce * 128, lds0 + ce * XC_B + (8 + (wave & 1)) * 1024);
        }
      }
    } else {
#pragma unroll
      for (int c = 0; c < 9; ++c) glds16_sc1(xo, s0 + c * 128, lds0 + c * XC_B + wave * 1024);
    }
  };
  const u64* foa0 = flg + F_OA + (has_tile ? rt0 * 16 : 0) + lane;
  const u64* foa1 = foa0 + (nrt == 5 ? 16 : 0);
  const int rq = rblk >> 3;
  const u64* fqkv = flg + F_QKV + ((h && (rblk & 7) < 2 && rq > 0) ? rq - 1 : rq) * 32 + (c32 < 27 ? c32 : 0);
  const u64* fwf = flg + F_WF + lane;
  const char* wfa = ws + WS_WF + (size_t)((has_tile ? cg * 4 : 0) + (wave & 3)) * 18432 + (wave >> 2) * 1024;
  const f32x16 z = {};
  f32x16 o = z;
  u32x4 wA[9];
  u32x4 wB[18], wE[3];
  const int cgB = rblk & 3, rB0 = (rblk >> 2) * 32;
  const char* wfb = ws + WS_WF + (size_t)(36 + cgB * 9 + wave) * 18432;
  const char* wfe = ws + WS_WF + (size_t)(36 + cgB * 9 + 8) * 18432 + wave * 1024;
  asm volatile("s_waitcnt vmcnt(0)" : "+v"(qf), "+v"(qrem)::"memory");
  lds_wait<4>(ctl + C_KV0);
  const float ms = max_pass(smem + ko, smem + kro, qf, rem_word(qrem, 0.f, h), 1);
  const unsigned rw = rem_word(qrem, bf_lo(pk2(ms + M_OFF, 0.f)), h);
  {
    int kq = ko, krq = kro, vq = vo;
    const char *kp = smem + kq, *krp = smem + krq, *vp = smem + vq;
    const s16x4 qe = mk2(rw, 0u), qo = mk2(0u, rw);
    s16x4 k01 = *(const s16x4*)krp, k23 = *(const s16x4*)(krp + 8);
    f32x16 sa = MF16(*(const bf16x8*)kp, qf, MF8(k01, qe, z)), sb;
    bf16x8 kf = *(const bf16x8*)(kp + 1024), v0 = *(const bf16x8*)vp, v1 = v0;
    u32x4 p0 = {}, p1 = {};
    u64 pf0, pf1;
    STAMP(3); CSTAMP(10);
#define WF9(W, BASE) asm volatile( \
        "global_load_dwordx4 %0, %9, %10 sc1\n\tglobal_load_dwordx4 %1, %9, %10 offset:2048 sc1\n\t" \
        "global_load_dwordx4 %2, %11, %10 offset:-4096 sc1\n\tglobal_load_dwordx4 %3, %11, %10 offset:-2048 sc1\n\t" \
        "global_load_dwordx4 %4, %11, %10 sc1\n\tglobal_load_dwordx4 %5, %11, %10 offset:2048 sc1\n\t" \
        "global_load_dwordx4 %6, %12, %10 offset:-4096 sc1\n\tglobal_load_dwordx4 %7, %12, %10 offset:-2048 sc1\n\t" \
        "global_load_dwordx4 %8, %12, %10 sc1" \
        : "=&v"(W[0]), "=&v"(W[1]), "=&v"(W[2]), "=&v"(W[3]), "=&v"(W[4]), "=&v"(W[5]), "=&v"(W[6]), "=&v"(W[7]), "=&v"(W[8]) \
        : "v"(ko), "s"(BASE), "v"(ko + 8192), "v"(ko + 16384) : "memory")
    auto group = [&](auto gc) __attribute__((always_inline)) {
      constexpr int g = decltype(gc)::value;
      LSTAMP(2 * g);
      if constexpr (PRIO(g) != PRIO(g - 1)) __builtin_amdgcn_s_setprio(PRIO(g));
      if constexpr (g == 1) { wait_vm<0>(); lds_add(ctl + C_KVA); kv_fetch(60 + wave, 8, 4); kv_fetch(92 + (wave & 3), 0, 1); POLL_ISSUE(pf0, fqkv); }
      if constexpr (g == 2) {
        lds_wait<8>(ctl + C_KVA);
        lds_wait<4>(ctl + C_STG);
        POLL_CHECK(pf0, fqkv, tok);
#pragma unroll
        for (int i = 0; i < 3; ++i) {
          int r = wave + 8 * i;
          r = r > 21 ? 21 : r;
          const int gr = rblk * 8 + r - 14;
          const char* src = ws + WS_QKV + (size_t)(gr < 0 ? 0 : gr) * (3 * ROWB) + ROWB;
          glds16_sc1(ko, src, lds0 + LA_0 + r * 2304);
          glds16_sc1(ko, src + 1024, lds0 + LA_0 + r * 2304 + 1024);
          glds16_part<0xffff>(ko, src + 2048, lds0 + LA_0 + r * 2304 + 2048);
        }
        const char* src = ws + WS_QKV + (size_t)row * (3 * ROWB);
        glds16_sc1(ko, src, lds0 + LA_Q + wave * ROWB);
        glds16_part<0xff>(ko, src + 1024, lds0 + LA_Q + wave * ROWB + 1024);
      }
      if constexpr (g == 3) { wait_vm<0>(); lds_add(ctl + C_AROWS); }
      if constexpr (g == 5) lds_wait<8>(ctl + C_AROWS);
      if constexpr (g == 4 || g == 5) {
        if ((wave >> 2) == g - 4) {
          lds_wait<8>(ctl + C_AROWS);
          local_attn(smem, lds0, wave, c32, h, row);
          store_row(smem + LA_Q + wave * ROWB, ws + WS_OA + (size_t)row * ROWB, lane);
          lds_add(ctl + C_A2);
        }
      }
      if constexpr (g == 6) { signal(flg + F_OA + row, tok); POLL_ISSUE(pf0, fwf); POLL_ISSUE(pf1, fwf + 44); }
      if constexpr (g == 7) {
        POLL_CHECK(pf0, fwf, tok);
        POLL_CHECK(pf1, fwf + 44, tok);
        lds_wait<8>(ctl + C_A2);
        kv_fetch(96 + wave, 8, 7);
      }
      if constexpr (g == 8) { wait_vm<0>(); lds_add(ctl + C_KVB); WF9(wA, wfa); }
      if constexpr (g == 9) lds_wait<8>(ctl + C_KVB);
      if constexpr (g == 9) { WF9((wB + 0), wfb); }
      if constexpr (g == 10) {
        WF9((wB + 9), wfb + 1024);
        asm volatile("global_load_dwordx4 %0, %3, %4 sc1\n\tglobal_load_dwordx4 %1, %5, %4 sc1\n\tglobal_load_dwordx4 %2, %6, %4 sc1"
                     : "=&v"(wE[0]), "=&v"(wE[1]), "=&v"(wE[2]) : "v"(ko), "s"(wfe), "v"(ko + 8192), "v"(wave < 2 ? ko + 16384 : ko) : "memory");
      }
      if constexpr (g == 11) lds_add(ctl + C_PAST);
      if constexpr (g == 12) { POLL_ISSUE(pf0, foa0); POLL_ISSUE(pf1, foa1); }
      if constexpr (g == 13) {
        lds_wait<8>(ctl + C_PAST);
        POLL_CHECK(pf0, foa0, tok);
        POLL_CHECK(pf1, foa1, tok);
        if (has_tile) fetch_tile(ws + WS_OA);
      }
      if constexpr (g == 15) { wait_vm<0>(); lds_add(ctl + C_XA); }
      LSTAMP(2 * g + 1);
      SB;
      const int G = (g & 3) * GRP_B;
      phase<true>(sa, sb, k01, krp + G + GRP_B, qo, kf, kp + G + 2048, qf, v0, vp + G + VT_B, v1, vp + G + 640, p0, p1, o);
      phase<false>(sb, sa, k23, nullptr, qe, kf, kp + G + 3072, qf, v0, vp + G + 2 * VT_B, v1, vp + G + VT_B + 640, p0, p1, o);
      phase<true>(sa, sb, k23, krp + G + GRP_B + 8, qo, kf, kp + G + GRP_B, qf, v0, vp + G + 3 * VT_B, v1, vp + G + 2 * VT_B + 640, p0, p1, o);
      phase<false>(sb, sa, k01, nullptr, qe, kf, kp + G + GRP_B + 1024, qf, v0, vp + G + GRP_B, v1, vp + G + 3 * VT_B + 640, p0, p1, o);
      asm volatile("" : "+v"(o), "+v"(sa), "+v"(p1));
      if constexpr ((g & 3) == 3) {
        kq += 4 * GRP_B; krq += 4 * GRP_B; vq += 4 * GRP_B;
        asm volatile("" : "+v"(kq), "+v"(krq), "+v"(vq));
        kp = smem + kq; krp = smem + krq; vp = smem + vq;
      }
    };
#define GR(i) group(IC<i>{});
    GR(0) GR(1) GR(2) GR(3) GR(4) GR(5) GR(6) GR(7) GR(8) GR(9) GR(10) GR(11) GR(12) GR(13) GR(14) GR(15)
    o = MF16(v1, __builtin_bit_cast(bf16x8, p1), o);
  }
  STAMP(4); CSTAMP(11); LSTAMP(32);
  float l = h ? o[12] : o[10];
  float big = l;
#pragma unroll
  for (int i = 0; i < 10; ++i) big = fmaxf(big, fabsf(o[i]));
  bool ok = (big < 1.0e37f) && (l > 0.f);
#pragma unroll
  for (int i = 0; i < 10; ++i) ok = ok && (o[i] == o[i]);
  if (!__all(ok)) {
    __builtin_amdgcn_fence(__ATOMIC_ACQUIRE, "agent");
    attn_exact(ws + WS_KV, ko, kro, vo, qf, qrem, h, o);
    l = h ? o[12] : o[10];
  }
  {
    const float inv = __builtin_amdgcn_rcpf(l);
    unsigned* dst = (unsigned*)(smem + L_OB + wave * ROWB + c32 * 36 + h * 8);
    dst[0] = pk2(o[0] * inv, o[1] * inv);
    dst[1] = pk2(o[2] * inv, o[3] * inv);
    dst[4] = pk2(o[4] * inv, o[5] * inv);
    dst[5] = pk2(o[6] * inv, o[7] * inv);
    if (!h) dst[8] = pk2(o[8] * inv, o[9] * inv);
    store_row(smem + L_OB + wave * ROWB, ws + WS_OB + (size_t)row * ROWB, lane);
  }
  const int xa = fr * 128 + ((((wave >> 2) * 4 + kg) ^ (fr & 7)) << 4);
  f32x4 acc[5];
  {
    bf16x8 xf[3][5];
    auto rdx = [&](int c) {
#pragma unroll
      for (int m = 0; m < 5; ++m) xf[c % 3][m] = *(const bf16x8*)(smem + c * XC_B + xa + m * 2048);
    };
    lds_wait<8>(ctl + C_XA);
    rdx(0); rdx(1);
#pragma unroll
    for (int c = 0; c < 9; ++c) {
      if (c + 2 < 9) rdx(c + 2);
      SB;
#pragma unroll
      for (int m = 0; m < 5; ++m) acc[m] = MFMA16(__builtin_bit_cast(bf16x8, wA[c]), xf[c % 3][m], c ? acc[m] : f32x4{});
      SB;
      if (c == SIGC) {
        signal(flg + F_OB + row, tok);
        STAMP(5); LSTAMP(33);
      }
    }
  }
  LSTAMP(34);
  bar_lgkm();
  {
    char* pw = smem + (wave >> 2) * P_HALF + fr * P_LD + (wave & 3) * 64 + kg * 16;
#pragma unroll
    for (int m = 0; m < 5; ++m) *(f32x4*)(pw + m * 16 * P_LD) = acc[m];
  }
  bar_lgkm();
  if (has_tile) {
#pragma unroll
    for (int i = 0; i < 2; ++i) {
      const int r = (tid >> 3) + 64 * i, c = tid & 7;
      if (r < nrt * 16) {
        const char* p = smem + r * P_LD + c * 32;
        const f32x4 a0 = *(const f32x4*)p, a1 = *(const f32x4*)(p + 16);
        const f32x4 b0 = *(const f32x4*)(p + P_HALF), b1 = *(const f32x4*)(p + P_HALF + 16);
        const u32x4 u = {pk2(a0[0] + b0[0], a0[1] + b0[1]), pk2(a0[2] + b0[2], a0[3] + b0[3]),
                         pk2(a1[0] + b1[0], a1[1] + b1[1]), pk2(a1[2] + b1[2], a1[3] + b1[3])};
        ST16(out + (size_t)(rt0 * 16 + r) * (2 * ROWB) + cg * 128 + c * 16, u);
      }
    }
  }
  if (wave >= 4) {
    if (wave == 4) {
      poll(flg + F_OB + rB0 + c32, tok);
      lds_add(ctl + C_GO);
    }
    lds_wait<1>(ctl + C_GO);
    LSTAMP(35);
    const char* s0 = ws + WS_OB + (size_t)(rB0 + (wave - 4) * 8) * ROWB;
#pragma unroll
    for (int c = 0; c < 9; ++c) glds16_sc1(xo, s0 + c * 128, lds0 + L_XB + c * 4096 + (wave - 4) * 1024);
    wait_vm<0>();
  } else if (rblk == 0 && wave == 0) {
#pragma unroll 1
    for (int i = 0; i < 4; ++i) poll(flg + F_ACK + i * 64 + lane, tok);
    signal(flg + F_EPOCH, tok - 0x9e3779b97f4a7c15ull + 1ull);
  }
  bar_lgkm();
  STAMP(6); LSTAMP(36);
  {
    const int xb0 = L_XB + fr * 128 + ((kg ^ (fr & 7)) << 4), xb1 = xb0 ^ 64;
    f32x4 b0 = {}, b1 = {}, b2 = {}, b3 = {}, e0 = {}, e1 = {};
    const int xe = ((wave & 1) ? xb1 : xb0) + (wave >> 1) * 4096;
    bf16x8 xq[2][12];
    auto rdb = [&](int t) {
#pragma unroll
      for (int i = 0; i < 12; ++i) xq[t & 1][i] = *(const bf16x8*)(smem + ((i & 2) ? xb1 : xb0) + (t * 3 + (i >> 2)) * 4096 + (i & 1) * 2048);
    };
    rdb(0);
#pragma unroll
    for (int t = 0; t < 3; ++t) {
      if (t < 2) rdb(t + 1);
      else {
#pragma unroll
        for (int i = 0; i < 6; ++i) xq[1][i] = *(const bf16x8*)(smem + xe + (i < 4 ? (i >> 1) * 16384 : (wave < 2 ? 32768 : 0)) + (i & 1) * 2048);
      }
      SB;
#pragma unroll
      for (int cc = 0; cc < 3; ++cc) {
        const int c = t * 3 + cc;
        b0 = MFMA16(__builtin_bit_cast(bf16x8, wB[c]), xq[t & 1][cc * 4], b0);
        b1 = MFMA16(__builtin_bit_cast(bf16x8, wB[c]), xq[t & 1][cc * 4 + 1], b1);
        b2 = MFMA16(__builtin_bit_cast(bf16x8, wB[9 + c]), xq[t & 1][cc * 4 + 2], b2);
        b3 = MFMA16(__builtin_bit_cast(bf16x8, wB[9 + c]), xq[t & 1][cc * 4 + 3], b3);
      }
      SB;
    }
    if (wave >= 2) wE[2] = u32x4{};
#pragma unroll
    for (int i = 0; i < 3; ++i) {
      e0 = MFMA16(__builtin_bit_cast(bf16x8, wE[i]), xq[1][2 * i], e0);
      e1 = MFMA16(__builtin_bit_cast(bf16x8, wE[i]), xq[1][2 * i + 1], e1);
    }
    b0 += b2; b1 += b3;
    char* tb = smem + L_TB + fr * TB_LD + wave * 32 + kg * 8;
    *(u32x2*)tb = u32x2{pk2(b0[0], b0[1]), pk2(b0[2], b0[3])};
    *(u32x2*)(tb + 16 * TB_LD) = u32x2{pk2(b1[0], b1[1]), pk2(b1[2], b1[3])};
    char* eb = smem + L_EB + wave * 2048 + fr * 64 + kg * 16;
    *(f32x4*)eb = e0;
    *(f32x4*)(eb + 1024) = e1;
  }
  LSTAMP(37);
  bar_lgkm();
  STAMP(8); LSTAMP(38);
  {
    const int r = tid >> 4, c = tid & 15;
    ST16(out + (size_t)(rB0 + r) * (2 * ROWB) + ROWB + cgB * 288 + c * 16, *(const u32x4*)(smem + L_TB + r * TB_LD + c * 16));
  }
  if (wave == 7) {
    const int r = lane >> 1, c = lane & 1;
    f32x4 s0 = {}, s1 = {};
#pragma unroll
    for (int w = 0; w < 8; ++w) {
      const char* p = smem + L_EB + w * 2048 + r * 64 + c * 32;
      s0 += *(const f32x4*)p; s1 += *(const f32x4*)(p + 16);
    }
    const u32x4 u = {pk2(s0[0], s0[1]), pk2(s0[2], s0[3]), pk2(s1[0], s1[1]), pk2(s1[2], s1[3])};
    ST16(out + (size_t)(rB0 + r) * (2 * ROWB) + ROWB + cgB * 288 + 256 + c * 16, u);
  }
  LSTAMP(39);
  STAMP(9);
  STAMP_OUT(1, ws); LSTAMP_OUT(ws);
}

extern "C" void kernel_launch(void* const* d_in, const int* in_sizes, int n_in, void* d_out, int out_size,
                              void* d_ws, size_t ws_size, hipStream_t stream) {
  (void)in_sizes; (void)n_in; (void)out_size; (void)ws_size;
  (void)hipFuncSetAttribute((const void*)k_all, hipFuncAttributeMaxDynamicSharedMemorySize, K2_LDS_ALL);
  k_all<<<256, 512, K2_LDS_ALL, stream>>>((const char*)d_in[0], (const char*)d_in[1], (const char*)d_in[2], (const char*)d_in[3],
                                          (const char*)d_in[4], (const char*)d_in[5], (const char*)d_in[6], (const char*)d_in[7],
                                          (char*)d_ws, (char*)d_out);
}
```

```cpp
#include <hip/hip_runtime.h>
#include <hip/hip_bf16.h>
#include <stdint.h>

typedef __bf16 bf16x2 __attribute__((ext_vector_type(2)));
typedef __bf16 bf16x8 __attribute__((ext_vector_type(8)));
typedef short s16x4 __attribute__((ext_vector_type(4)));
typedef float f32x2 __attribute__((ext_vector_type(2)));
typedef float f32x4 __attribute__((ext_vector_type(4)));
typedef float f32x16 __attribute__((ext_vector_type(16)));
typedef unsigned u32x2 __attribute__((ext_vector_type(2)));
typedef unsigned u32x4 __attribute__((ext_vector_type(4)));
#define LDSP(T) __attribute__((address_space(3))) T*
#define DI __device__ __forceinline__
#define STAMP_DECL
#define STAMP(i) do {} while (0)
#define CSTAMP(i) do {} while (0)
#define LSTAMP(i) do {} while (0)
#define LSTAMP_OUT(w) do {} while (0)
#define STAMP_ARG
#define STAMP_PASS
#define STAMP_OUT(kid, wsp) do {} while (0)

#define K1UNROLL _Pragma("unroll")
#define PRIO(g) ((g) < 0 ? -1 : 3 - (((g) >> 1) & 3))
#define POLL_SLEEP "4"
#define SIGC 3
constexpr int SEQ = 2048, DM = 576;
constexpr int ROWB = DM * 2;
constexpr float LOG2E = 1.4426950408889634f;
constexpr float QSCALE = 0.23570226039551584f * LOG2E;

constexpr int GRP_B = 9728, KR_OFF = 4096, V_OFF = 4608, VT_B = 1280;
constexpr int KV_BYTES = 16 * GRP_B;
constexpr size_t WS_KV = 0;
constexpr size_t WS_QB = WS_KV + KV_BYTES;
constexpr size_t WS_QKV = WS_QB + (size_t)SEQ * ROWB;
constexpr size_t WS_OA = WS_QKV + (size_t)SEQ * 3 * ROWB;
constexpr size_t WS_OB = WS_OA + (size_t)SEQ * ROWB;
constexpr size_t WS_WF = WS_OB + (size_t)SEQ * ROWB;
constexpr size_t WS_F = WS_WF + (size_t)72 * 18432;
constexpr int F_OA = 0, F_KV = 2048, F_Q = F_KV + 64, F_EPOCH = F_Q + 64 * 16, F_ACK = F_EPOCH + 16, F_QKV = F_ACK + 256,
              F_WF = F_QKV + 32 * 32, F_OB = F_WF + 128;
typedef unsigned long long u64;

template <int N> struct IC { static constexpr int value = N; };
DI unsigned pk2(float a, float b) {
  f32x2 f = {a, b};
  return __builtin_bit_cast(unsigned, __builtin_convertvector(f, bf16x2));
}
DI float dot2(unsigned a, unsigned b, float c) {
  return __builtin_amdgcn_fdot2_f32_bf16(__builtin_bit_cast(bf16x2, a), __builtin_bit_cast(bf16x2, b), c, false);
}
DI float bf_lo(unsigned u) { return __uint_as_float(u << 16); }
DI float bf_hi(unsigned u) { return __uint_as_float(u & 0xffff0000u); }

DI void glds16(unsigned voff, const void* sbase, unsigned lds_dst) {
  unsigned keep;
  asm volatile("s_mov_b32 %0, m0\n\ts_mov_b32 m0, %3\n\ts_nop 0\n\tglobal_load_lds_dwordx4 %1, %2\n\ts_mov_b32 m0, %0"
               : "=&s"(keep) : "v"(voff), "s"(sbase), "s"(lds_dst) : "memory");
}
DI void glds4(unsigned voff, const void* sbase, unsigned lds_dst) {
  unsigned keep;
  asm volatile("s_mov_b32 %0, m0\n\ts_mov_b32 m0, %3\n\ts_nop 0\n\tglobal_load_lds_dword %1, %2\n\ts_mov_b32 m0, %0"
               : "=&s"(keep) : "v"(voff), "s"(sbase), "s"(lds_dst) : "memory");
}
template <int N> DI void wait_vm() { asm volatile("s_waitcnt vmcnt(%0)" ::"i"(N) : "memory"); }
DI void bar_lgkm() { asm volatile("s_waitcnt lgkmcnt(0)\n\ts_barrier" ::: "memory"); }
DI bf16x8 tr_frag(const char* p) {
  s16x4 lo = __builtin_amdgcn_ds_read_tr16_b64_v4i16((LDSP(s16x4))(p));
  s16x4 hi = __builtin_amdgcn_ds_read_tr16_b64_v4i16((LDSP(s16x4))(p + 256));
  return __builtin_bit_cast(bf16x8, __builtin_shufflevector(lo, hi, 0, 1, 2, 3, 4, 5, 6, 7));
}
#define ST16(p, v) __builtin_nontemporal_store(v, (u32x4*)(p))
#define MFMA16(a, b, c) __builtin_amdgcn_mfma_f32_16x16x32_bf16(a, b, c, 0, 0, 0)

constexpr int K2_LDS = KV_BYTES + 6144;
constexpr float M_OFF = 96.0f;
#define SB __builtin_amdgcn_sched_barrier(0)
#define MF16(a, b, c) __builtin_amdgcn_mfma_f32_32x32x16_bf16(a, b, c, 0, 0, 0)
#define MF8(a, b, c) __builtin_amdgcn_mfma_f32_32x32x8bf16_1k(a, b, c, 0, 0, 0)
#define EX(i) cur[i] = __builtin_amdgcn_exp2f(cur[i])

DI float xhalf(float x);
DI float max16(const f32x16& s, float m) {
#pragma unroll
  for (int i = 0; i < 16; i += 2) m = __builtin_fmaxf(__builtin_fmaxf(s[i], s[i + 1]), m);
  return m;
}
#define E(i) EX(i); SB;
#define C0(i) p0[i] = pk2(cur[2 * i], cur[2 * i + 1]); SB;
#define C1(i) p1[i] = pk2(cur[8 + 2 * i], cur[9 + 2 * i]); SB;
template <bool RELOAD>
DI void phase(f32x16& cur, f32x16& nxt, s16x4& kra, const char* krp, const s16x4 qr, bf16x8& kf, const char* kp2, const bf16x8 qf,
              bf16x8& v0, const char* v0p, bf16x8& v1, const char* v1p, u32x4& p0, u32x4& p1, f32x16& o) {
  const f32x16 z = {};
  nxt = MF8(kra, qr, z); SB;
  if (RELOAD) { kra = *(const s16x4*)krp; SB; }
  E(0) E(1) E(2) E(3) C0(0) E(4)
  o = MF16(v1, __builtin_bit_cast(bf16x8, p1), o); SB;
  v1 = *(const bf16x8*)v1p; SB;
  C0(1) E(5) E(6) E(7) C0(2) E(8)
  nxt = MF16(kf, qf, nxt); SB;
  kf = *(const bf16x8*)kp2; SB;
  C0(3) E(9) E(10) E(11) C1(0) E(12)
  o = MF16(v0, __builtin_bit_cast(bf16x8, p0), o); SB;
  v0 = *(const bf16x8*)v0p; SB;
  C1(1) E(13) E(14) E(15) C1(2) C1(3)
}
DI unsigned rem_word(unsigned qrem, float m, int h) {
  const unsigned hi = pk2(-m, 0.f) & 0xffffu;
  const unsigned lo = pk2(-m - bf_lo(hi), 0.f) & 0xffffu;
  return h ? (hi | (lo << 16)) : qrem;
}
DI s16x4 mk2(unsigned a, unsigned b) { u32x2 r = {a, b}; return __builtin_bit_cast(s16x4, r); }

DI float max_pass(const char* kp, const char* krp, const bf16x8 qf, unsigned rw, int npair) {
  const s16x4 qe = mk2(rw, 0u), qo = mk2(0u, rw);
  const f32x16 z = {};
  float m = -3.0e38f;
  for (int i = 0; i < npair; ++i) {
    const int G = (i >> 1) * GRP_B, hf = i & 1;
    const s16x4 kr = *(const s16x4*)(krp + G + hf * 8);
    m = max16(MF16(*(const bf16x8*)(kp + G + hf * 2048), qf, MF8(kr, qe, z)), m);
    m = max16(MF16(*(const bf16x8*)(kp + G + hf * 2048 + 1024), qf, MF8(kr, qo, z)), m);
  }
  return fmaxf(m, xhalf(m));
}
DI void attn_exact(const char* img, int ko, int kro, int vo, const bf16x8 qf, unsigned qrem, int h, f32x16& o) {
  const float mt = max_pass(img + ko, img + kro, qf, rem_word(qrem, 0.f, h), 32);
  const unsigned rw = rem_word(qrem, mt, h);
  const s16x4 qe = mk2(rw, 0u), qo = mk2(0u, rw);
  const f32x16 z = {};
  o = z;
  for (int t = 0; t < 64; ++t) {
    const char* g = img + (t >> 2) * GRP_B;
    const s16x4 kr = *(const s16x4*)(g + kro + (t & 2) * 4);
    f32x16 cur = MF16(*(const bf16x8*)(g + ko + (t & 3) * 1024), qf, MF8(kr, (t & 1) ? qo : qe, z));
    u32x4 p0, p1;
#pragma unroll
    for (int i = 0; i < 16; ++i) EX(i);
#pragma unroll
    for (int i = 0; i < 4; ++i) { p0[i] = pk2(cur[2 * i], cur[2 * i + 1]); p1[i] = pk2(cur[8 + 2 * i], cur[9 + 2 * i]); }
    o = MF16(*(const bf16x8*)(g + vo + (t & 3) * VT_B), __builtin_bit_cast(bf16x8, p0), o);
    o = MF16(*(const bf16x8*)(g + vo + (t & 3) * VT_B + 640), __builtin_bit_cast(bf16x8, p1), o);
  }
}

constexpr int LA_0 = 98304;
constexpr int LA_Q = LA_0 + 22 * 2304;
constexpr int XC_B = 10240;
constexpr int L_OB = 92160;
constexpr int L_XB = 102400;
constexpr int P_LD = 272, P_HALF = 80 * P_LD;
constexpr int L_TB = 2 * P_HALF, TB_LD = 272;
constexpr int L_EB = L_TB + 32 * TB_LD;
constexpr int L_CTL = K2_LDS;
constexpr int K2_LDS_ALL = K2_LDS + 64;
enum { C_KVA = 0, C_KVB = 4, C_PAST = 8, C_AROWS = 12, C_A2 = 16, C_XA = 20, C_KV0 = 24, C_GO = 28, C_GO1 = 32, C_STG = 36, C_TOK = 40 };

DI void lds_add(unsigned addr) {
  asm volatile("s_waitcnt lgkmcnt(0)\n\ts_mov_b64 exec, 1\n\tds_add_u32 %0, %1\n\ts_mov_b64 exec, -1" ::"v"(addr), "v"(1u) : "memory");
}
template <int N> DI void lds_wait(unsigned addr) {
  unsigned r, t;
  asm volatile("1:\n\tds_read_b32 %0, %2\n\ts_waitcnt lgkmcnt(0)\n\tv_readfirstlane_b32 %1, %0\n\ts_cmp_lt_u32 %1, %3\n\ts_cbranch_scc1 1b"
               : "=&v"(r), "=&s"(t) : "v"(addr), "i"(N) : "memory", "scc");
}
DI float xhalf(float x) {
  const unsigned u = __float_as_uint(x);
  const auto r = __builtin_amdgcn_permlane32_swap(u, u, false, false);
  return __uint_as_float((threadIdx.x & 32) ? r[0] : r[1]);
}
DI void local_attn(char* smem, unsigned lds0, int wave, int c32, int h, int row) {
  unsigned* qp = (unsigned*)(smem + LA_Q + wave * ROWB + c32 * 36);
  unsigned q[9];
#pragma unroll
  for (int e = 0; e < 9; ++e) q[e] = qp[e];
  const unsigned kb = lds0 + LA_0 + (wave + 8 - h * 8) * 2304 + c32 * 36;
  const unsigned vb = lds0 + LA_0 + wave * 2304 + ROWB + c32 * 36 + h * 16;
  u32x2 kk[9][2], vv[5][4];
#pragma unroll
  for (int e = 0; e < 9; ++e)
    asm volatile("ds_read2st64_b32 %0, %2 offset0:54 offset1:36\n\tds_read2st64_b32 %1, %2 offset0:18 offset1:0"
                 : "=&v"(kk[e][0]), "=&v"(kk[e][1]) : "v"(kb + e * 4) : "memory");
  asm volatile("s_waitcnt lgkmcnt(0)" : "+v"(kk[0][0]), "+v"(kk[0][1]), "+v"(kk[1][0]), "+v"(kk[1][1]), "+v"(kk[2][0]), "+v"(kk[2][1]),
      "+v"(kk[3][0]), "+v"(kk[3][1]), "+v"(kk[4][0]), "+v"(kk[4][1]), "+v"(kk[5][0]), "+v"(kk[5][1]), "+v"(kk[6][0]), "+v"(kk[6][1]),
      "+v"(kk[7][0]), "+v"(kk[7][1]), "+v"(kk[8][0]), "+v"(kk[8][1])::"memory");
#pragma unroll
  for (int e = 0; e < 5; ++e)
    asm volatile("ds_read2st64_b32 %0, %4 offset0:126 offset1:108\n\tds_read2st64_b32 %1, %4 offset0:90 offset1:72\n\t"
                 "ds_read2st64_b32 %2, %4 offset0:54 offset1:36\n\tds_read2st64_b32 %3, %4 offset0:18 offset1:0"
                 : "=&v"(vv[e][0]), "=&v"(vv[e][1]), "=&v"(vv[e][2]), "=&v"(vv[e][3]) : "v"(vb + e * 4) : "memory");
  float lg[4] = {0.f, 0.f, 0.f, 0.f};
#pragma unroll
  for (int e = 0; e < 9; ++e) {
    const unsigned k0 = kk[e][0][0], k1 = kk[e][0][1], k2 = kk[e][1][0], k3 = kk[e][1][1];
    lg[0] = dot2(q[e], k0, lg[0]); lg[1] = dot2(q[e], k1, lg[1]); lg[2] = dot2(q[e], k2, lg[2]); lg[3] = dot2(q[e], k3, lg[3]);
  }
#pragma unroll
  for (int w = 0; w < 4; ++w) lg[w] = row - 2 * (h * 4 + w) >= 0 ? lg[w] * QSCALE : -1.0e9f * LOG2E;
  float mx = fmaxf(fmaxf(lg[0], lg[1]), fmaxf(lg[2], lg[3]));
  mx = fmaxf(mx, xhalf(mx));
  float sum = 0.f;
#pragma unroll
  for (int w = 0; w < 4; ++w) { lg[w] = __builtin_amdgcn_exp2f(lg[w] - mx); sum += lg[w]; }
  sum += xhalf(sum);
  const float inv = __builtin_amdgcn_rcpf(sum);
  const unsigned pa = pk2(lg[0] * inv, lg[1] * inv), pb = pk2(lg[2] * inv, lg[3] * inv);
  const auto ra = __builtin_amdgcn_permlane32_swap(pa, pa, false, false);
  const auto rb = __builtin_amdgcn_permlane32_swap(pb, pb, false, false);
  const unsigned pp[4] = {ra[0], rb[0], ra[1], rb[1]};
  asm volatile("s_waitcnt lgkmcnt(0)" : "+v"(vv[0][0]), "+v"(vv[0][1]), "+v"(vv[0][2]), "+v"(vv[0][3]), "+v"(vv[1][0]), "+v"(vv[1][1]),
      "+v"(vv[1][2]), "+v"(vv[1][3]), "+v"(vv[2][0]), "+v"(vv[2][1]), "+v"(vv[2][2]), "+v"(vv[2][3]), "+v"(vv[3][0]), "+v"(vv[3][1]),
      "+v"(vv[3][2]), "+v"(vv[3][3]), "+v"(vv[4][0]), "+v"(vv[4][1]), "+v"(vv[4][2]), "+v"(vv[4][3])::"memory");
#pragma unroll
  for (int e = 0; e < 5; ++e) {
    float o0 = 0.f, o1 = 0.f;
#pragma unroll
    for (int w2 = 0; w2 < 4; ++w2) {
      const unsigned x = vv[e][w2][0], y = vv[e][w2][1];
      o0 = dot2(__builtin_amdgcn_perm(y, x, 0x05040100u), pp[w2], o0);
      o1 = dot2(__builtin_amdgcn_perm(y, x, 0x07060302u), pp[w2], o1);
    }
    qp[h * 4 + e] = pk2(o0, o1);
  }
}
template <int MASK> DI void glds16_part(unsigned voff, const void* sbase, unsigned lds_dst) {
  unsigned keep;
  asm volatile("s_mov_b32 %0, m0\n\ts_mov_b32 m0, %3\n\ts_mov_b64 exec, %4\n\tglobal_load_lds_dwordx4 %1, %2 sc1\n\ts_mov_b64 exec, -1\n\ts_mov_b32 m0, %0"
               : "=&s"(keep) : "v"(voff), "s"(sbase), "s"(lds_dst), "i"(MASK) : "memory");
}
DI void glds16_sc1(unsigned voff, const void* sbase, unsigned lds_dst) {
  unsigned keep;
  asm volatile("s_mov_b32 %0, m0\n\ts_mov_b32 m0, %3\n\ts_nop 0\n\tglobal_load_lds_dwordx4 %1, %2 sc1\n\ts_mov_b32 m0, %0"
               : "=&s"(keep) : "v"(voff), "s"(sbase), "s"(lds_dst) : "memory");
}
DI void store_row(const char* stage, char* dst, int lane) {
  asm volatile("s_waitcnt lgkmcnt(0)" ::: "memory");
  const u32x4 a = *(const u32x4*)(stage + lane * 16);
  const u32x4 b = *(const u32x4*)(stage + 1024 + (lane & 7) * 16);
  asm volatile("global_store_dwordx4 %0, %1, off sc1\n\ts_nop 1" ::"v"(dst + lane * 16), "v"(a) : "memory");
  if (lane < 8) asm volatile("global_store_dwordx4 %0, %1, off offset:1024 sc1\n\ts_nop 1" ::"v"(dst + lane * 16), "v"(b) : "memory");
}
DI void st16_sc1(char* p, u32x4 v) { asm volatile("global_store_dwordx4 %0, %1, off sc1\n\ts_nop 1" ::"v"(p), "v"(v) : "memory"); }
DI void st4_sc1(char* p, unsigned v) { asm volatile("global_store_dword %0, %1, off sc1" ::"v"(p), "v"(v) : "memory"); }
DI void signal(u64* f, u64 tok) {
  asm volatile("s_waitcnt vmcnt(0)\n\ts_mov_b64 exec, 1\n\tglobal_store_dwordx2 %0, %1, off sc1\n\ts_mov_b64 exec, -1" ::"v"(f), "v"(tok) : "memory");
}
DI void poll(const u64* p, u64 tok) {
  u64 r; unsigned n;
  asm volatile("s_mov_b32 %1, 0x200000\n1:\n\tglobal_load_dwordx2 %0, %2, off sc1\n\ts_sub_u32 %1, %1, 1\n\ts_cbranch_scc1 2f\n\t"
               "s_waitcnt vmcnt(0)\n\tv_cmp_ne_u64 vcc, %3, %0\n\ts_cbranch_vccz 2f\n\ts_sleep " POLL_SLEEP "\n\ts_branch 1b\n2:\n\ts_waitcnt vmcnt(0)"
               : "=&v"(r), "=&s"(n) : "v"(p), "s"(tok) : "memory", "scc", "vcc");
}
#define POLL_ISSUE(r, p) asm volatile("global_load_dwordx2 %0, %1, off sc1" : "=v"(r) : "v"(p) : "memory")
#define POLL_CHECK(r, p, tok) do { asm volatile("s_waitcnt vmcnt(0)" : "+v"(r)::"memory"); if (!__all((r) == (tok))) poll(p, tok); } while (0)

constexpr int G_XB = 16384, G_STAGE = 32768, G_RING = 30720;
constexpr int L_S1 = 0, L_S2 = LA_0, ST_LD = 144;

template <int KIND> struct Loader {
  static constexpr int NX = KIND == 2 ? 4 : (KIND == 3 ? 0 : 2), P = NX + (KIND == 1 ? 10 : 4);
  unsigned xo[4], wo[4], eo[4];
  const char *X, *W0, *W1, *wk, *wv;
  int ldw, ks, l;
  unsigned lds0;
  DI void init(const char* X_, int row0, const char* W0_, const char* W1_, int ldw_, int cb0, int cb1, const char* wk_, const char* wv_,
               int ks0, unsigned lds0_, int l_, int lane) {
    X = X_; W0 = W0_; W1 = W1_; wk = wk_; wv = wv_; ldw = ldw_; ks = ks0; lds0 = lds0_; l = l_;
#pragma unroll
    for (int i = 0; i < 4; ++i) {
      xo[i] = (row0 + (l + 4 * i) * 8 + (lane >> 3)) * ROWB + (((lane & 7) ^ (lane >> 3)) << 4);
      const int kp = l + 4 * (i >> 1);
      wo[i] = (kp * 8 + ((lane >> 2) & 7)) * ldw + ((i & 1) ? cb1 : cb0) + (lane >> 5) * 64 + (((lane & 3) ^ ((kp & 1) << 1)) << 4);
      int dw = (lane & 15) ^ ((kp & 1) << 3);
      dw = dw > 8 ? 8 : dw;
      eo[i] = (kp * 8 + (i & 1) * 4 + (lane >> 4)) * 36 + dw * 4;
    }
  }
  DI void issue(int t) {
    const unsigned sb = lds0 + (t & 3) * G_STAGE;
#pragma unroll
    for (int i = 0; i < NX; ++i) glds16(xo[i], X + ks * 128, sb + (l + 4 * i) * 1024);
    const char* w0 = W0 + (size_t)ks * 64 * ldw;
    glds16(wo[0], w0, sb + G_XB + l * 2048);
    glds16(wo[2], w0, sb + G_XB + (l + 4) * 2048);
    if (KIND != 1) {
      const char* w1 = W1 + (size_t)ks * 64 * ldw;
      glds16(wo[1], w1, sb + G_XB + l * 2048 + 1024);
      glds16(wo[3], w1, sb + G_XB + (l + 4) * 2048 + 1024);
    } else {
#pragma unroll
      for (int i = 0; i < 4; ++i) {
        const unsigned d = sb + G_XB + (l + 4 * (i >> 1)) * 2048 + 1024 + (i & 1) * 256;
        glds4(eo[i], wk + ks * (64 * 36), d);
        glds4(eo[i], wv + ks * (64 * 36), d + 512);
      }
    }
    ks = ks == 8 ? 0 : ks + 1;
  }
  DI void first() {
    issue(0); issue(1);
    wait_vm<P>(); bar_lgkm();
    issue(2);
#pragma unroll 1
    for (int t = 3; t < 9; ++t) { issue(t); wait_vm<2 * P>(); bar_lgkm(); }
  }
  template <int PPREV, int N> DI void second() {
    issue(9); wait_vm<P + PPREV>(); bar_lgkm();
#pragma unroll 1
    for (int t = 10; t < 9 + N; ++t) { issue(t); wait_vm<2 * P>(); bar_lgkm(); }
  }
  template <class H1, class H2> DI void last(H1 h1, H2 h2) { wait_vm<P>(); bar_lgkm(); h1(); wait_vm<0>(); bar_lgkm(); h2(); bar_lgkm(); }
};

template <int MT, class HOOK>
DI void gemm_job(const char* smem, int s0, int wm, int trE, int trO, int fr, int kg, f32x4 (&acc)[MT][4], HOOK hook) {
  const int xo0 = (wm * MT * 16 + fr) * 128 + ((kg ^ (fr & 7)) << 4), xo1 = xo0 ^ 64;
  bf16x8 xa[MT], wa[4], xb[MT], wb[4];
  auto rd = [&](const char* sb, int kk, bf16x8 (&xf)[MT], bf16x8 (&wf)[4]) {
#pragma unroll
    for (int m = 0; m < MT; ++m) xf[m] = *(const bf16x8*)(sb + (kk ? xo1 : xo0) + m * 2048);
#pragma unroll
    for (int n = 0; n < 4; ++n) wf[n] = tr_frag(sb + ((n & 1) ? trO : trE) + kk * 8192 + (n >> 1) * 512);
  };
  auto mm = [&](bf16x8 (&xf)[MT], bf16x8 (&wf)[4]) {
#pragma unroll
    for (int n = 0; n < 4; ++n)
#pragma unroll
      for (int m = 0; m < MT; ++m) acc[m][n] = MFMA16(wf[n], xf[m], acc[m][n]);
  };
  auto ilv = [&] {
    constexpr int NR = MT + 8, NM = 4 * MT, RPM = NM >= NR ? 1 : 2;
#pragma unroll
    for (int q = 0; q < (NR + RPM - 1) / RPM; ++q) { __builtin_amdgcn_sched_group_barrier(0x8, 1, 0); __builtin_amdgcn_sched_group_barrier(0x100, RPM, 0); }
    __builtin_amdgcn_sched_group_barrier(0x8, NM, 0);
    __builtin_amdgcn_sched_barrier(0);
  };
  rd(smem + (s0 & 3) * G_STAGE, 0, xa, wa);
#pragma unroll
  for (int j = 0; j < 9; ++j) {
    rd(smem + ((s0 + j) & 3) * G_STAGE, 1, xb, wb);
    mm(xa, wa);
    ilv();
    bar_lgkm();
    if (j == 1) hook();
    if (j < 8) rd(smem + ((s0 + j + 1) & 3) * G_STAGE, 0, xa, wa);
    mm(xb, wb);
    ilv();
  }
}
template <int MT>
DI void stage_tile(char* st, f32x4 (&acc)[MT][4], bool scale, int fr, int kg) {
#pragma unroll
  for (int m = 0; m < MT; ++m)
#pragma unroll
    for (int n = 0; n < 4; ++n) {
      u32x2 u = {pk2(acc[m][n][0], acc[m][n][1]), pk2(acc[m][n][2], acc[m][n][3])};
      if (scale) {
        u[0] = pk2(bf_lo(u[0]) * QSCALE, bf_hi(u[0]) * QSCALE);
        u[1] = pk2(bf_lo(u[1]) * QSCALE, bf_hi(u[1]) * QSCALE);
      }
      *(u32x2*)(st + (m * 16 + fr) * ST_LD + n * 32 + kg * 8) = u;
    }
  asm volatile("s_waitcnt lgkmcnt(0)" ::: "memory");
}
template <int MT>
DI void store_tile(const char* st, char* dst, int ld, int lane) {
  u32x4 v[MT * 2];
#pragma unroll
  for (int i = 0; i < MT * 2; ++i) v[i] = *(const u32x4*)(st + (i * 8 + (lane >> 3)) * ST_LD + (lane & 7) * 16);
  char* d = dst + (size_t)(lane >> 3) * ld + (lane & 7) * 16;
#pragma unroll
  for (int i = 0; i < MT * 2; ++i) st16_sc1(d + (size_t)i * 8 * ld, v[i]);
}

__global__ __launch_bounds__(512) void k_all(const char* __restrict__ xA, const char* __restrict__ wqkv, const char* __restrict__ woA,
                                             const char* __restrict__ xB, const char* __restrict__ wq, const char* __restrict__ wk,
                                             const char* __restrict__ wv, const char* __restrict__ woB,
                                             char* __restrict__ ws, char* __restrict__ out) {
  extern __shared__ __attribute__((aligned(16))) char smem[];
  const int tid = threadIdx.x, lane = tid & 63;
  const int wave = __builtin_amdgcn_readfirstlane(tid >> 6);
  STAMP_DECL; STAMP(0);
  const int b = blockIdx.x;
  const int xg = b & 7, xi = b >> 3;
  const int rblk = xg * 32 + xi;
  const int row = rblk * 8 + wave;
  const int c32 = lane & 31, h = lane >> 5;
  const int fr = lane & 15, kg = lane >> 4;
  const unsigned lds0 = (unsigned)(uintptr_t)smem;
  const unsigned ctl = lds0 + L_CTL;
  const int ko = lane * 16;
  u64* const flg = (u64*)(ws + WS_F);
  u64 tok;
  bool pre = false;
  auto kv_fetch = [&](int p0, int step, int n) {
#pragma unroll
    for (int i = 0; i < n; ++i) glds16_sc1(ko, ws + WS_KV + (p0 + step * i) * 1024, lds0 + (p0 + step * i) * 1024);
  };
  {
    const bool has1 = xi < 20;
    const int rb1 = xg * 4 + (xi & 3), ct1 = 4 - (xi >> 2);
    const bool has2 = xi >= 4;
    const int rb2 = (xg >> 1) * 4 + (xi & 3), ct2 = (xg & 1) * 7 + 7 - (xi >> 2);
    const int ru = xi * 8 + xg;
    const bool hasr = xi < 4 && ru < 27;
    const int rl = ru / 3, rk = (ru - rl * 3) * 3;
    const int rot = rblk % 9;
    if (wave >= 4) {
      const int l = wave - 4;
      u64 pe0, pe1;
      auto early1 = [&] {
        POLL_ISSUE(pe0, flg + F_KV + lane);
        POLL_ISSUE(pe1, flg + F_Q + (row >> 5) * 16 + (lane < 9 ? lane : 0));
      };
      auto early2 = [&] {
        asm volatile("" : "+v"(pe0), "+v"(pe1));
        const u64 tk = *(const u64*)(smem + L_CTL + C_TOK);
        if (__all(pe0 == tk && pe1 == tk)) { pre = true; lds_add(ctl + C_GO1); kv_fetch(l, 4, 15); }
      };
      Loader<2> g2;
      if (has2) g2.init(xA, rb2 * 128, wqkv, wqkv, 3 * ROWB, ct2 * 256, ct2 * 256 + (ct2 == 13 ? 0 : 128), nullptr, nullptr, rot, lds0 + G_RING, l, lane);
      if (!has1) { g2.first(); g2.last([] {}, [] {}); }
      else if (has2) {
        Loader<0> g1;
        g1.init(xB, rb1 * 64, wq, wq, ROWB, ct1 * 256, ct1 * 256 + 128, nullptr, nullptr, rot, lds0 + G_RING, l, lane);
        g1.first();
        g2.second<6, 9>();
        g2.last(early1, early2);
      } else {
        Loader<1> g1;
        g1.init(xB, rb1 * 64, wq, wq, ROWB, 1024, 0, wk, wv, rot, lds0 + G_RING, l, lane);
        g1.first();
        if (hasr) {
          const int c0 = rl * 128, c1 = c0 + 64;
          Loader<3> g3;
          g3.init(nullptr, 0, c0 < DM ? woA : woB, c1 < DM ? woA : woB, ROWB, (c0 < DM ? c0 : c0 - DM) * 2, (c1 < DM ? c1 : c1 - DM) * 2,
                  nullptr, nullptr, rk, lds0 + G_RING, l, lane);
          g3.second<12, 3>();
          g3.last([] {}, [] {});
        } else g1.last([] {}, [] {});
      }
      tok = *(const u64*)(smem + L_CTL + C_TOK);
    } else {
      if (wave == 0) {
        u64 e;
        asm volatile("global_load_dwordx2 %0, %1, off sc1\n\ts_waitcnt vmcnt(0)" : "=&v"(e) : "v"(flg + F_EPOCH) : "memory");
        e += 0x9e3779b97f4a7c15ull;
        if (lane < 10) *(unsigned*)(smem + L_CTL + lane * 4) = 0u;
        if (lane == 0) *(u64*)(smem + L_CTL + C_TOK) = e;
        signal(flg + F_ACK + rblk, e);
      }
      const int wm = wave & 1, wn = wave >> 1;
      const int tro = G_XB + kg * 2048 + (fr >> 2) * 64 + (fr & 3) * 8;
      const int trE = tro + ((kg & 1) << 5), trO = tro + (((kg & 1) ^ 1) << 5);
      bar_lgkm();
      STAMP(1);
      tok = *(const u64*)(smem + L_CTL + C_TOK);
      u64* const fq1 = flg + F_Q + (rb1 * 2 + wm) * 16 + ct1 * 2 + wn;
      if (has1) {
        f32x4 acc[2][4] = {};
        gemm_job<2>(smem + G_RING, 0, wm, trE + wn * 1024, trO + wn * 1024, fr, kg, acc, [] { wait_vm<0>(); });
        char* st = smem + L_S1 + wave * (32 * ST_LD);
        const bool kvw = ct1 == 4 && wn == 1;
        stage_tile<2>(st, acc, !kvw, fr, kg);
        const int t = rb1 * 2 + wm;
        if (!kvw) {
          store_tile<2>(st, ws + WS_QB + (size_t)t * 32 * ROWB + ct1 * 256 + wn * 128, ROWB, lane);
          if (!has2) signal(fq1, tok);
        } else {
          char* g = ws + WS_KV + (t >> 2) * GRP_B;
          st16_sc1(g + (t & 3) * 1024 + ko, *(const u32x4*)(st + c32 * ST_LD + h * 16));
          if (!h) st4_sc1(g + KR_OFF + c32 * 16 + (t & 3) * 4, *(const unsigned*)(st + c32 * ST_LD + 32));
#pragma unroll
          for (int i = 0; i < 2; ++i) {
            const int sl = lane + 64 * i;
            if (sl < 80) {
              const int blk = sl / 20, d = sl - blk * 20;
              u32x4 o;
              if (d < 18) {
                const char* src = st + ((blk >> 1) * 16 + (blk & 1) * 4) * ST_LD + 64 + d * 2;
#pragma unroll
                for (int e = 0; e < 4; ++e) {
                  const int r = (e >> 1) * 8 + (e & 1) * 2;
                  o[e] = (unsigned)*(const unsigned short*)(src + r * ST_LD) | ((unsigned)*(const unsigned short*)(src + (r + 1) * ST_LD) << 16);
                }
              } else {
                const unsigned f = d == 18 ? 0x3f803f80u : 0u;
                o = u32x4{f, f, f, f};
              }
              st16_sc1(g + V_OFF + (t & 3) * VT_B + sl * 16, o);
            }
          }
          signal(flg + F_KV + t, tok);
        }
      }
      STAMP(2);
      const int s0 = has1 ? 9 : 0;
      if (has2) {
        f32x4 acc[4][4] = {};
        gemm_job<4>(smem + G_RING, s0, wm, trE + wn * 1024, trO + wn * 1024, fr, kg, acc, [&] { wait_vm<0>(); if (has1) signal(fq1, tok); });
        STAMP(7);
        if (!(ct2 == 13 && wn == 1)) {
          char* st = smem + L_S2 + wave * (64 * ST_LD);
          stage_tile<4>(st, acc, false, fr, kg);
          const int t = rb2 * 2 + wm;
          store_tile<4>(st, ws + WS_QKV + (size_t)t * 64 * (3 * ROWB) + ct2 * 256 + wn * 128, 3 * ROWB, lane);
          signal(flg + F_QKV + t * 32 + ct2 * 2 + wn, tok);
        }
      } else if (hasr) {
        char* dst = ws + WS_WF + (size_t)(rl * 8 + wave * 2) * 18432 + rk * 2048 + ko;
#pragma unroll 1
        for (int j = 0; j < 3; ++j) {
          const char* sb = smem + G_RING + ((9 + j) & 3) * G_STAGE + wave * 512;
          const bf16x8 f0 = tr_frag(sb + trE), f1 = tr_frag(sb + trO), f2 = tr_frag(sb + trE + 8192), f3 = tr_frag(sb + trO + 8192);
          bar_lgkm();
          st16_sc1(dst + j * 2048, __builtin_bit_cast(u32x4, f0));
          st16_sc1(dst + j * 2048 + 18432, __builtin_bit_cast(u32x4, f1));
          st16_sc1(dst + j * 2048 + 1024, __builtin_bit_cast(u32x4, f2));
          st16_sc1(dst + j * 2048 + 18432 + 1024, __builtin_bit_cast(u32x4, f3));
        }
        signal(flg + F_WF + ru * 4 + wave, tok);
      }
      lds_add(ctl + C_STG);
    }
  }
  tok = ((u64)(unsigned)__builtin_amdgcn_readfirstlane((unsigned)(tok >> 32)) << 32) | (unsigned)__builtin_amdgcn_readfirstlane((unsigned)tok);
  bf16x8 qf;
  unsigned qrem;
  if (wave == 4 && !pre) {
    poll(flg + F_KV + lane, tok);
    poll(flg + F_Q + (row >> 5) * 16 + (lane < 9 ? lane : 0), tok);
    lds_add(ctl + C_GO1);
  }
  lds_wait<1>(ctl + C_GO1);
  if (wave >= 4 && !pre) {
    if (xi < 4) lds_wait<4>(ctl + C_STG);
    kv_fetch(wave - 4, 4, 15);
  }
  {
    const char* qp = ws + WS_QB + (size_t)row * ROWB + c32 * 36;
    asm volatile("global_load_dwordx4 %0, %1, off sc1" : "=v"(qf) : "v"(qp + h * 16) : "memory");
    asm volatile("global_load_dword %0, %1, off offset:32 sc1" : "=v"(qrem) : "v"(qp) : "memory");
  }
  if (wave >= 4) { wait_vm<9>(); lds_add(ctl + C_KV0); }
  const int vrow = c32 < 19 ? c32 : (c32 == 28 ? 18 : 19);
  const int kro = h ? V_OFF + 18 * 16 : KR_OFF + c32 * 16;
  const int vo = V_OFF + h * 320 + vrow * 16;
  const int rg = rblk / 9, cg = rblk - rg * 9;
  const int nrt = rg < 16 ? 5 : 4, rt0 = rg < 16 ? rg * 5 : 16 + rg * 4;
  const bool has_tile = rblk < 252;
  const unsigned xo = (lane >> 3) * ROWB + (((lane & 7) ^ (lane >> 3)) << 4);
  auto fetch_tile = [&](const char* src) {
    const char* s0 = src + (size_t)(rt0 * 16 + wave * 8) * ROWB;
    const char* s1 = src + (size_t)(rt0 * 16 + 64 + (wave & 1) * 8) * ROWB;
    if (nrt == 5) {
#pragma unroll
      for (int c = 0; c < 9; ++c) {
        glds16_sc1(xo, s0 + c * 128, lds0 + c * XC_B + wave * 1024);
        if (c % 3 == 2) {
          const int ce = c == 8 ? 8 : (c / 3) * 4 + (wave >> 1);
          glds16_sc1(xo, s1 + ce * 128, lds0 + ce * XC_B + (8 + (wave & 1)) * 1024);
        }
      }
    } else {
#pragma unroll
      for (int c = 0; c < 9; ++c) glds16_sc1(xo, s0 + c * 128, lds0 + c * XC_B + wave * 1024);
    }
  };
  const u64* foa0 = flg + F_OA + (has_tile ? rt0 * 16 : 0) + lane;
  const u64* foa1 = foa0 + (nrt == 5 ? 16 : 0);
  const int rq = rblk >> 3;
  const u64* fqkv = flg + F_QKV + ((h && (rblk & 7) < 2 && rq > 0) ? rq - 1 : rq) * 32 + (c32 < 27 ? c32 : 0);
  const u64* fwf = flg + F_WF + lane;
  const char* wfa = ws + WS_WF + (size_t)((has_tile ? cg * 4 : 0) + (wave & 3)) * 18432 + (wave >> 2) * 1024;
  const f32x16 z = {};
  f32x16 o = z;
  u32x4 wA[9];
  u32x4 wB[18], wE[3];
  const int cgB = rblk & 3, rB0 = (rblk >> 2) * 32;
  const char* wfb = ws + WS_WF + (size_t)(36 + cgB * 9 + wave) * 18432;
  const char* wfe = ws + WS_WF + (size_t)(36 + cgB * 9 + 8) * 18432 + wave * 1024;
  asm volatile("s_waitcnt vmcnt(0)" : "+v"(qf), "+v"(qrem)::"memory");
  lds_wait<4>(ctl + C_KV0);
  const float ms = max_pass(smem + ko, smem + kro, qf, rem_word(qrem, 0.f, h), 1);
  const unsigned rw = rem_word(qrem, bf_lo(pk2(ms + M_OFF, 0.f)), h);
  {
    int kq = ko, krq = kro, vq = vo;
    const char *kp = smem + kq, *krp = smem + krq, *vp = smem + vq;
    const s16x4 qe = mk2(rw, 0u), qo = mk2(0u, rw);
    s16x4 k01 = *(const s16x4*)krp, k23 = *(const s16x4*)(krp + 8);
    f32x16 sa = MF16(*(const bf16x8*)kp, qf, MF8(k01, qe, z)), sb;
    bf16x8 kf = *(const bf16x8*)(kp + 1024), v0 = *(const bf16x8*)vp, v1 = v0;
    u32x4 p0 = {}, p1 = {};
    u64 pf0, pf1;
    STAMP(3); CSTAMP(10);
#define WF9(W, BASE) asm volatile( \
        "global_load_dwordx4 %0, %9, %10 sc1\n\tglobal_load_dwordx4 %1, %9, %10 offset:2048 sc1\n\t" \
        "global_load_dwordx4 %2, %11, %10 offset:-4096 sc1\n\tglobal_load_dwordx4 %3, %11, %10 offset:-2048 sc1\n\t" \
        "global_load_dwordx4 %4, %11, %10 sc1\n\tglobal_load_dwordx4 %5, %11, %10 offset:2048 sc1\n\t" \
        "global_load_dwordx4 %6, %12, %10 offset:-4096 sc1\n\tglobal_load_dwordx4 %7, %12, %10 offset:-2048 sc1\n\t" \
        "global_load_dwordx4 %8, %12, %10 sc1" \
        : "=&v"(W[0]), "=&v"(W[1]), "=&v"(W[2]), "=&v"(W[3]), "=&v"(W[4]), "=&v"(W[5]), "=&v"(W[6]), "=&v"(W[7]), "=&v"(W[8]) \
        : "v"(ko), "s"(BASE), "v"(ko + 8192), "v"(ko + 16384) : "memory")
    auto group = [&](auto gc) __attribute__((always_inline)) {
      constexpr int g = decltype(gc)::value;
      LSTAMP(2 * g);
      if constexpr (PRIO(g) != PRIO(g - 1)) __builtin_amdgcn_s_setprio(PRIO(g));
      if constexpr (g == 1) { wait_vm<0>(); lds_add(ctl + C_KVA); kv_fetch(60 + wave, 8, 4); kv_fetch(92 + (wave & 3), 0, 1); POLL_ISSUE(pf0, fqkv); }
      if constexpr (g == 2) {
        lds_wait<8>(ctl + C_KVA);
        lds_wait<4>(ctl + C_STG);
        POLL_CHECK(pf0, fqkv, tok);
#pragma unroll
        for (int i = 0; i < 3; ++i) {
          int r = wave + 8 * i;
          r = r > 21 ? 21 : r;
          const int gr = rblk * 8 + r - 14;
          const char* src = ws + WS_QKV + (size_t)(gr < 0 ? 0 : gr) * (3 * ROWB) + ROWB;
          glds16_sc1(ko, src, lds0 + LA_0 + r * 2304);
          glds16_sc1(ko, src + 1024, lds0 + LA_0 + r * 2304 + 1024);
          glds16_part<0xffff>(ko, src + 2048, lds0 + LA_0 + r * 2304 + 2048);
        }
        const char* src = ws + WS_QKV + (size_t)row * (3 * ROWB);
        glds16_sc1(ko, src, lds0 + LA_Q + wave * ROWB);
        glds16_part<0xff>(ko, src + 1024, lds0 + LA_Q + wave * ROWB + 1024);
      }
      if constexpr (g == 3) { wait_vm<0>(); lds_add(ctl + C_AROWS); }
      if constexpr (g == 5) lds_wait<8>(ctl + C_AROWS);
      if constexpr (g == 4 || g == 5) {
        if ((wave >> 2) == g - 4) {
          lds_wait<8>(ctl + C_AROWS);
          local_attn(smem, lds0, wave, c32, h, row);
          store_row(smem + LA_Q + wave * ROWB, ws + WS_OA + (size_t)row * ROWB, lane);
          lds_add(ctl + C_A2);
        }
      }
      if constexpr (g == 6) { signal(flg + F_OA + row, tok); POLL_ISSUE(pf0, fwf); POLL_ISSUE(pf1, fwf + 44); }
      if constexpr (g == 7) {
        POLL_CHECK(pf0, fwf, tok);
        POLL_CHECK(pf1, fwf + 44, tok);
        lds_wait<8>(ctl + C_A2);
        kv_fetch(96 + wave, 8, 7);
      }
      if constexpr (g == 8) { wait_vm<0>(); lds_add(ctl + C_KVB); WF9(wA, wfa); }
      if constexpr (g == 9) lds_wait<8>(ctl + C_KVB);
      if constexpr (g == 9) { WF9((wB + 0), wfb); }
      if constexpr (g == 10) {
        WF9((wB + 9), wfb + 1024);
        asm volatile("global_load_dwordx4 %0, %3, %4 sc1\n\tglobal_load_dwordx4 %1, %5, %4 sc1\n\tglobal_load_dwordx4 %2, %6, %4 sc1"
                     : "=&v"(wE[0]), "=&v"(wE[1]), "=&v"(wE[2]) : "v"(ko), "s"(wfe), "v"(ko + 8192), "v"(wave < 2 ? ko + 16384 : ko) : "memory");
      }
      if constexpr (g == 11) lds_add(ctl + C_PAST);
      if constexpr (g == 12) { POLL_ISSUE(pf0, foa0); POLL_ISSUE(pf1, foa1); }
      if constexpr (g == 13) {
        lds_wait<8>(ctl + C_PAST);
        POLL_CHECK(pf0, foa0, tok);
        POLL_CHECK(pf1, foa1, tok);
        if (has_tile) fetch_tile(ws + WS_OA);
      }
      if constexpr (g == 15) { wait_vm<0>(); lds_add(ctl + C_XA); }
      LSTAMP(2 * g + 1);
      SB;
      const int G = (g & 3) * GRP_B;
      phase<true>(sa, sb, k01, krp + G + GRP_B, qo, kf, kp + G + 2048, qf, v0, vp + G + VT_B, v1, vp + G + 640, p0, p1, o);
      phase<false>(sb, sa, k23, nullptr, qe, kf, kp + G + 3072, qf, v0, vp + G + 2 * VT_B, v1, vp + G + VT_B + 640, p0, p1, o);
      phase<true>(sa, sb, k23, krp + G + GRP_B + 8, qo, kf, kp + G + GRP_B, qf, v0, vp + G + 3 * VT_B, v1, vp + G + 2 * VT_B + 640, p0, p1, o);
      phase<false>(sb, sa, k01, nullptr, qe, kf, kp + G + GRP_B + 1024, qf, v0, vp + G + GRP_B, v1, vp + G + 3 * VT_B + 640, p0, p1, o);
      asm volatile("" : "+v"(o), "+v"(sa), "+v"(p1));
      if constexpr ((g & 3) == 3) {
        kq += 4 * GRP_B; krq += 4 * GRP_B; vq += 4 * GRP_B;
        asm volatile("" : "+v"(kq), "+v"(krq), "+v"(vq));
        kp = smem + kq; krp = smem + krq; vp = smem + vq;
      }
    };
#define GR(i) group(IC<i>{});
    GR(0) GR(1) GR(2) GR(3) GR(4) GR(5) GR(6) GR(7) GR(8) GR(9) GR(10) GR(11) GR(12) GR(13) GR(14) GR(15)
    o = MF16(v1, __builtin_bit_cast(bf16x8, p1), o);
  }
  STAMP(4); CSTAMP(11); LSTAMP(32);
  float l = h ? o[12] : o[10];
  float big = l;
#pragma unroll
  for (int i = 0; i < 10; ++i) big = fmaxf(big, fabsf(o[i]));
  bool ok = (big < 1.0e37f) && (l > 0.f);
#pragma unroll
  for (int i = 0; i < 10; ++i) ok = ok && (o[i] == o[i]);
  if (!__all(ok)) {
    __builtin_amdgcn_fence(__ATOMIC_ACQUIRE, "agent");
    attn_exact(ws + WS_KV, ko, kro, vo, qf, qrem, h, o);
    l = h ? o[12] : o[10];
  }
  {
    const float inv = __builtin_amdgcn_rcpf(l);
    unsigned* dst = (unsigned*)(smem + L_OB + wave * ROWB + c32 * 36 + h * 8);
    dst[0] = pk2(o[0] * inv, o[1] * inv);
    dst[1] = pk2(o[2] * inv, o[3] * inv);
    dst[4] = pk2(o[4] * inv, o[5] * inv);
    dst[5] = pk2(o[6] * inv, o[7] * inv);
    if (!h) dst[8] = pk2(o[8] * inv, o[9] * inv);
    store_row(smem + L_OB + wave * ROWB, ws + WS_OB + (size_t)row * ROWB, lane);
  }
  const int xa = fr * 128 + ((((wave >> 2) * 4 + kg) ^ (fr & 7)) << 4);
  f32x4 acc[5];
  {
    bf16x8 xf[3][5];
    auto rdx = [&](int c) {
#pragma unroll
      for (int m = 0; m < 5; ++m) xf[c % 3][m] = *(const bf16x8*)(smem + c * XC_B + xa + m * 2048);
    };
    lds_wait<8>(ctl + C_XA);
    rdx(0); rdx(1);
#pragma unroll
    for (int c = 0; c < 9; ++c) {
      if (c + 2 < 9) rdx(c + 2);
      SB;
#pragma unroll
      for (int m = 0; m < 5; ++m) acc[m] = MFMA16(__builtin_bit_cast(bf16x8, wA[c]), xf[c % 3][m], c ? acc[m] : f32x4{});
      SB;
      if (c == SIGC) {
        signal(flg + F_OB + row, tok);
        STAMP(5); LSTAMP(33);
      }
    }
  }
  LSTAMP(34);
  bar_lgkm();
  {
    char* pw = smem + (wave >> 2) * P_HALF + fr * P_LD + (wave & 3) * 64 + kg * 16;
#pragma unroll
    for (int m = 0; m < 5; ++m) *(f32x4*)(pw + m * 16 * P_LD) = acc[m];
  }
  bar_lgkm();
  if (has_tile) {
#pragma unroll
    for (int i = 0; i < 2; ++i) {
      const int r = (tid >> 3) + 64 * i, c = tid & 7;
      if (r < nrt * 16) {
        const char* p = smem + r * P_LD + c * 32;
        const f32x4 a0 = *(const f32x4*)p, a1 = *(const f32x4*)(p + 16);
        const f32x4 b0 = *(const f32x4*)(p + P_HALF), b1 = *(const f32x4*)(p + P_HALF + 16);
        const u32x4 u = {pk2(a0[0] + b0[0], a0[1] + b0[1]), pk2(a0[2] + b0[2], a0[3] + b0[3]),
                         pk2(a1[0] + b1[0], a1[1] + b1[1]), pk2(a1[2] + b1[2], a1[3] + b1[3])};
        ST16(out + (size_t)(rt0 * 16 + r) * (2 * ROWB) + cg * 128 + c * 16, u);
      }
    }
  }
  if (wave >= 4) {
    if (wave == 4) {
      poll(flg + F_OB + rB0 + c32, tok);
      lds_add(ctl + C_GO);
    }
    lds_wait<1>(ctl + C_GO);
    LSTAMP(35);
    const char* s0 = ws + WS_OB + (size_t)(rB0 + (wave - 4) * 8) * ROWB;
#pragma unroll
    for (int c = 0; c < 9; ++c) glds16_sc1(xo, s0 + c * 128, lds0 + L_XB + c * 4096 + (wave - 4) * 1024);
    wait_vm<0>();
  } else if (rblk == 0 && wave == 0) {
#pragma unroll 1
    for (int i = 0; i < 4; ++i) poll(flg + F_ACK + i * 64 + lane, tok);
    signal(flg + F_EPOCH, tok - 0x9e3779b97f4a7c15ull + 1ull);
  }
  bar_lgkm();
  STAMP(6); LSTAMP(36);
  {
    const int xb0 = L_XB + fr * 128 + ((kg ^ (fr & 7)) << 4), xb1 = xb0 ^ 64;
    f32x4 b0 = {}, b1 = {}, b2 = {}, b3 = {}, e0 = {}, e1 = {};
    const int xe = ((wave & 1) ? xb1 : xb0) + (wave >> 1) * 4096;
    bf16x8 xq[2][12];
    auto rdb = [&](int t) {
#pragma unroll
      for (int i = 0; i < 12; ++i) xq[t & 1][i] = *(const bf16x8*)(smem + ((i & 2) ? xb1 : xb0) + (t * 3 + (i >> 2)) * 4096 + (i & 1) * 2048);
    };
    rdb(0);
#pragma unroll
    for (int t = 0; t < 3; ++t) {
      if (t < 2) rdb(t + 1);
      else {
#pragma unroll
        for (int i = 0; i < 6; ++i) xq[1][i] = *(const bf16x8*)(smem + xe + (i < 4 ? (i >> 1) * 16384 : (wave < 2 ? 32768 : 0)) + (i & 1) * 2048);
      }
      SB;
#pragma unroll
      for (int cc = 0; cc < 3; ++cc) {
        const int c = t * 3 + cc;
        b0 = MFMA16(__builtin_bit_cast(bf16x8, wB[c]), xq[t & 1][cc * 4], b0);
        b1 = MFMA16(__builtin_bit_cast(bf16x8, wB[c]), xq[t & 1][cc * 4 + 1], b1);
        b2 = MFMA16(__builtin_bit_cast(bf16x8, wB[9 + c]), xq[t & 1][cc * 4 + 2], b2);
        b3 = MFMA16(__builtin_bit_cast(bf16x8, wB[9 + c]), xq[t & 1][cc * 4 + 3], b3);
      }
      SB;
    }
    if (wave >= 2) wE[2] = u32x4{};
#pragma unroll
    for (int i = 0; i < 3; ++i) {
      e0 = MFMA16(__builtin_bit_cast(bf16x8, wE[i]), xq[1][2 * i], e0);
      e1 = MFMA16(__builtin_bit_cast(bf16x8, wE[i]), xq[1][2 * i + 1], e1);
    }
    b0 += b2; b1 += b3;
    char* tb = smem + L_TB + fr * TB_LD + wave * 32 + kg * 8;
    *(u32x2*)tb = u32x2{pk2(b0[0], b0[1]), pk2(b0[2], b0[3])};
    *(u32x2*)(tb + 16 * TB_LD) = u32x2{pk2(b1[0], b1[1]), pk2(b1[2], b1[3])};
    char* eb = smem + L_EB + wave * 2048 + fr * 64 + kg * 16;
    *(f32x4*)eb = e0;
    *(f32x4*)(eb + 1024) = e1;
  }
  LSTAMP(37);
  bar_lgkm();
  STAMP(8); LSTAMP(38);
  {
    const int r = tid >> 4, c = tid & 15;
    ST16(out + (size_t)(rB0 + r) * (2 * ROWB) + ROWB + cgB * 288 + c * 16, *(const u32x4*)(smem + L_TB + r * TB_LD + c * 16));
  }
  if (wave == 7) {
    const int r = lane >> 1, c = lane & 1;
    f32x4 s0 = {}, s1 = {};
#pragma unroll
    for (int w = 0; w < 8; ++w) {
      const char* p = smem + L_EB + w * 2048 + r * 64 + c * 32;
      s0 += *(const f32x4*)p; s1 += *(const f32x4*)(p + 16);
    }
    const u32x4 u = {pk2(s0[0], s0[1]), pk2(s0[2], s0[3]), pk2(s1[0], s1[1]), pk2(s1[2], s1[3])};
    ST16(out + (size_t)(rB0 + r) * (2 * ROWB) + ROWB + cgB * 288 + 256 + c * 16, u);
  }
  LSTAMP(39);
  STAMP(9);
  STAMP_OUT(1, ws); LSTAMP_OUT(ws);
}

extern "C" void kernel_launch(void* const* d_in, const int* in_sizes, int n_in, void* d_out, int out_size,
                              void* d_ws, size_t ws_size, hipStream_t stream) {
  (void)in_sizes; (void)n_in; (void)out_size; (void)ws_size;
  (void)hipFuncSetAttribute((const void*)k_all, hipFuncAttributeMaxDynamicSharedMemorySize, K2_LDS_ALL);
  k_all<<<256, 512, K2_LDS_ALL, stream>>>((const char*)d_in[0], (const char*)d_in[1], (const char*)d_in[2], (const char*)d_in[3],
                                          (const char*)d_in[4], (const char*)d_in[5], (const char*)d_in[6], (const char*)d_in[7],
                                          (char*)d_ws, (char*)d_out);
}
```

```cpp
#include <hip/hip_runtime.h>
#include <hip/hip_bf16.h>
#include <stdint.h>

typedef __bf16 bf16x2 __attribute__((ext_vector_type(2)));
typedef __bf16 bf16x8 __attribute__((ext_vector_type(8)));
typedef short s16x4 __attribute__((ext_vector_type(4)));
typedef float f32x2 __attribute__((ext_vector_type(2)));
typedef float f32x4 __attribute__((ext_vector_type(4)));
typedef float f32x16 __attribute__((ext_vector_type(16)));
typedef unsigned u32x2 __attribute__((ext_vector_type(2)));
typedef unsigned u32x4 __attribute__((ext_vector_type(4)));
#define LDSP(T) __attribute__((address_space(3))) T*
#define DI __device__ __forceinline__
#define STAMP_DECL
#define STAMP(i) do {} while (0)
#define CSTAMP(i) do {} while (0)
#define LSTAMP(i) do {} while (0)
#define LSTAMP_OUT(w) do {} while (0)
#define STAMP_ARG
#define STAMP_PASS
#define STAMP_OUT(kid, wsp) do {} while (0)

#define K1UNROLL _Pragma("unroll")
#define PRIO(g) ((g) < 0 ? -1 : 3 - (((g) >> 1) & 3))
#define POLL_SLEEP "4"
#define SIGC 3
constexpr int SEQ = 2048, DM = 576;
constexpr int ROWB = DM * 2;
constexpr float LOG2E = 1.4426950408889634f;
constexpr float QSCALE = 0.23570226039551584f * LOG2E;

constexpr int GRP_B = 9728, KR_OFF = 4096, V_OFF = 4608, VT_B = 1280;
constexpr int KV_BYTES = 16 * GRP_B;
constexpr size_t WS_KV = 0;
constexpr size_t WS_QB = WS_KV + KV_BYTES;
constexpr size_t WS_QKV = WS_QB + (size_t)SEQ * ROWB;
constexpr size_t WS_OA = WS_QKV + (size_t)SEQ * 3 * ROWB;
constexpr size_t WS_OB = WS_OA + (size_t)SEQ * ROWB;
constexpr size_t WS_WF = WS_OB + (size_t)SEQ * ROWB;
constexpr size_t WS_CNT = WS_WF + (size_t)72 * 18432;

template <int N> struct IC { static constexpr int value = N; };
DI unsigned pk2(float a, float b) {
  f32x2 f = {a, b};
  return __builtin_bit_cast(unsigned, __builtin_convertvector(f, bf16x2));
}
DI float dot2(unsigned a, unsigned b, float c) {
  return __builtin_amdgcn_fdot2_f32_bf16(__builtin_bit_cast(bf16x2, a), __builtin_bit_cast(bf16x2, b), c, false);
}
DI float bf_lo(unsigned u) { return __uint_as_float(u << 16); }
DI float bf_hi(unsigned u) { return __uint_as_float(u & 0xffff0000u); }

DI void glds16(unsigned voff, const void* sbase, unsigned lds_dst) {
  unsigned keep;
  asm volatile("s_mov_b32 %0, m0\n\ts_mov_b32 m0, %3\n\ts_nop 0\n\tglobal_load_lds_dwordx4 %1, %2\n\ts_mov_b32 m0, %0"
               : "=&s"(keep) : "v"(voff), "s"(sbase), "s"(lds_dst) : "memory");
}
DI void glds4(unsigned voff, const void* sbase, unsigned lds_dst) {
  unsigned keep;
  asm volatile("s_mov_b32 %0, m0\n\ts_mov_b32 m0, %3\n\ts_nop 0\n\tglobal_load_lds_dword %1, %2\n\ts_mov_b32 m0, %0"
               : "=&s"(keep) : "v"(voff), "s"(sbase), "s"(lds_dst) : "memory");
}
template <int N> DI void wait_vm() { asm volatile("s_waitcnt vmcnt(%0)" ::"i"(N) : "memory"); }
DI void bar_lgkm() { asm volatile("s_waitcnt lgkmcnt(0)\n\ts_barrier" ::: "memory"); }
DI bf16x8 tr_frag(const char* p) {
  s16x4 lo = __builtin_amdgcn_ds_read_tr16_b64_v4i16((LDSP(s16x4))(p));
  s16x4 hi = __builtin_amdgcn_ds_read_tr16_b64_v4i16((LDSP(s16x4))(p + 256));
  return __builtin_bit_cast(bf16x8, __builtin_shufflevector(lo, hi, 0, 1, 2, 3, 4, 5, 6, 7));
}
#define ST16(p, v) __builtin_nontemporal_store(v, (u32x4*)(p))
DI void ST16K(char* p, u32x4 v) { asm volatile("global_store_dwordx4 %0, %1, off sc1\n\ts_nop 1" :: "v"(p), "v"(v) : "memory"); }
#define OUTST ST16K
#define MFMA16(a, b, c) __builtin_amdgcn_mfma_f32_16x16x32_bf16(a, b, c, 0, 0, 0)

constexpr int K1_XB = 20480, K1_STAGE = 36864, K1_LDS = 4 * K1_STAGE;
constexpr int K1_RB = 13, K1_ACT = 14, K1_BCT = 5, K1_GEMM = K1_RB * (K1_ACT + K1_BCT);

template <int MODE>
DI void k1_loader(const char* X, int row0, const char* W0, const char* W1, int ldw, int colB0, int colB1,
                  const char* wk, const char* wv, int rot, unsigned lds0, int l, int lane) {
  unsigned xo[5], wo[4], eo[8];
  if (MODE != 2) {
#pragma unroll
    for (int i = 0; i < 5; ++i) {
      int r = row0 + (l + 4 * i) * 8 + (lane >> 3);
      r = r > SEQ - 1 ? SEQ - 1 : r;
      xo[i] = r * ROWB + (((lane & 7) ^ (lane >> 3)) << 4);
    }
  }
#pragma unroll
  for (int i = 0; i < 4; ++i) {
    const int kp = l + 4 * (i >> 1);
    const int k = kp * 8 + ((lane >> 2) & 7);
    wo[i] = k * ldw + ((i & 1) ? colB1 : colB0) + (lane >> 5) * 64 + (((lane & 3) ^ ((kp & 1) << 1)) << 4);
  }
  if (MODE == 1) {
#pragma unroll
    for (int i = 0; i < 4; ++i) {
      const int kp = l + 4 * (i >> 1);
      const int k = kp * 8 + (i & 1) * 4 + (lane >> 4);
      int dw = (lane & 15) ^ ((kp & 1) << 3);
      dw = dw > 8 ? 8 : dw;
      eo[i] = k * 36 + dw * 4;
    }
  }
  auto issue = [&](int j) {
    int ks = j + rot; ks = ks >= 9 ? ks - 9 : ks;
    const unsigned sb = lds0 + (j & 3) * K1_STAGE;
    if (MODE != 2) {
      const char* xb = X + ks * 128;
#pragma unroll
      for (int i = 0; i < 5; ++i) glds16(xo[i], xb, sb + (l + 4 * i) * 1024);
    }
    const char* w0 = W0 + (size_t)ks * 64 * ldw;
    const char* w1 = W1 + (size_t)ks * 64 * ldw;
#pragma unroll
    for (int i = 0; i < 4; ++i) {
      if (MODE == 1 && (i & 1)) continue;
      glds16(wo[i], (i & 1) ? w1 : w0, sb + K1_XB + (l + 4 * (i >> 1)) * 2048 + (i & 1) * 1024);
    }
    if (MODE == 1) {
      const char* kb = wk + ks * (64 * 36);
      const char* vb = wv + ks * (64 * 36);
#pragma unroll
      for (int i = 0; i < 4; ++i) {
        const unsigned d = sb + K1_XB + (l + 4 * (i >> 1)) * 2048 + 1024 + (i & 1) * 256;
        glds4(eo[i], kb, d);
        glds4(eo[i], vb, d + 512);
      }
    }
  };
  constexpr int P = MODE == 0 ? 9 : (MODE == 1 ? 15 : 4);
#pragma unroll 1
  for (int t = 0; t < 9; ++t) {
    issue(t);
    if (t == 1) { wait_vm<P>(); bar_lgkm(); }
    else if (t >= 3) { wait_vm<2 * P>(); bar_lgkm(); }
  }
  wait_vm<P>(); bar_lgkm();
  wait_vm<0>(); bar_lgkm();
  bar_lgkm();
}

__global__ __launch_bounds__(512) void k1_proj(const char* __restrict__ xA, const char* __restrict__ wqkv,
                                               const char* __restrict__ xB, const char* __restrict__ wq,
                                               const char* __restrict__ wk, const char* __restrict__ wv,
                                               const char* __restrict__ woA, const char* __restrict__ woB,
                                               char* __restrict__ ws) {
  extern __shared__ __attribute__((aligned(16))) char smem[];
  asm volatile("" ::"s"(xA), "s"(wqkv), "s"(woA), "s"(xB), "s"(wq), "s"(wk), "s"(wv), "s"(woB), "s"(ws));
  const int tid = threadIdx.x, lane = tid & 63;
  const int wave = __builtin_amdgcn_readfirstlane(tid >> 6);
  STAMP_DECL; STAMP(0);
  const unsigned lds0 = (unsigned)(uintptr_t)smem;
  const int b = blockIdx.x;
  const int unit = (b & 7) * 32 + (b >> 3);
  const int fr = lane & 15, kg = lane >> 4;
  const int rot = unit % 9;
  const int tro = kg * 2048 + (fr >> 2) * 64 + (fr & 3) * 8;
  const int trE = K1_XB + tro + ((kg & 1) << 5), trO = K1_XB + tro + (((kg & 1) ^ 1) << 5);

  if (__builtin_expect(unit >= K1_GEMM, 0)) {
    const int s = unit - K1_GEMM;
    if (s == 0 && tid < 256) *(unsigned*)(ws + WS_CNT + tid * 128) = 0u;
    if (wave >= 4) {
      const int c0 = s * 128, c1 = c0 + 64;
      k1_loader<2>(nullptr, 0, c0 < DM ? woA : woB, c1 < DM ? woA : woB, ROWB, (c0 < DM ? c0 : c0 - DM) * 2, (c1 < DM ? c1 : c1 - DM) * 2,
                   nullptr, nullptr, rot, lds0, wave - 4, lane);
    } else {
      bar_lgkm();
      char* dst = ws + WS_WF + (size_t)(s * 8 + wave * 2) * 18432 + lane * 16;
#pragma unroll 1
      for (int j = 0; j < 9; ++j) {
        int ks = j + rot; ks = ks >= 9 ? ks - 9 : ks;
        const char* sb = smem + (j & 3) * K1_STAGE + wave * 512;
        const bf16x8 f0 = tr_frag(sb + trE), f1 = tr_frag(sb + trO), f2 = tr_frag(sb + trE + 8192), f3 = tr_frag(sb + trO + 8192);
        bar_lgkm();
        ST16(dst + ks * 2048, __builtin_bit_cast(u32x4, f0));
        ST16(dst + ks * 2048 + 18432, __builtin_bit_cast(u32x4, f1));
        ST16(dst + ks * 2048 + 1024, __builtin_bit_cast(u32x4, f2));
        ST16(dst + ks * 2048 + 18432 + 1024, __builtin_bit_cast(u32x4, f3));
      }
    }
    STAMP(9);
    STAMP_OUT(0, ws);
    return;
  }
  const bool isA = unit < K1_RB * K1_ACT;
  int rb, ct;
  if (isA) {
    if (unit < 70) { ct = unit / 5; rb = unit - ct * 5; }
    else { const int u = unit - 70, band = u >= 56 ? 1 : 0, v = u - band * 56; ct = v >> 2; rb = 5 + band * 4 + (v & 3); }
  } else { const int u = unit - K1_RB * K1_ACT; rb = u / K1_BCT; ct = u - rb * K1_BCT; }
  const int row0 = rb * 160;
  const bool last = ct == (isA ? K1_ACT - 1 : K1_BCT - 1);
  const int wm = wave & 1, wn = (wave >> 1) & 1;
  constexpr int ST_LD = 144;
  if (__builtin_expect(wave >= 4, 1)) {
    if (__builtin_expect(isA || !last, 1))
      k1_loader<0>(isA ? xA : xB, row0, isA ? wqkv : wq, isA ? wqkv : wq, isA ? 3 * ROWB : ROWB, ct * 256, ct * 256 + (last ? 0 : 128),
                   nullptr, nullptr, rot, lds0, wave - 4, lane);
    else k1_loader<1>(xB, row0, wq, wq, ROWB, ct * 256, 0, wk, wv, rot, lds0, wave - 4, lane);
  } else {
    f32x4 acc[5][4] = {};
    const int xo0 = (wm * 80 + fr) * 128 + ((kg ^ (fr & 7)) << 4), xo1 = xo0 ^ 64;
    const int wE = trE + wn * 1024, wO = trO + wn * 1024;
    bf16x8 xa[5], wa[4], xb[5], wb[4];
    auto rd = [&](const char* sb, int kk, bf16x8 (&xf)[5], bf16x8 (&wf)[4]) {
#pragma unroll
      for (int m = 0; m < 5; ++m) xf[m] = *(const bf16x8*)(sb + (kk ? xo1 : xo0) + m * 2048);
#pragma unroll
      for (int n = 0; n < 4; ++n) wf[n] = tr_frag(sb + ((n & 1) ? wO : wE) + kk * 8192 + (n >> 1) * 512);
    };
    auto mm = [&](bf16x8 (&xf)[5], bf16x8 (&wf)[4]) {
#pragma unroll
      for (int n = 0; n < 4; ++n)
#pragma unroll
        for (int m = 0; m < 5; ++m) acc[m][n] = MFMA16(wf[n], xf[m], acc[m][n]);
    };
    bar_lgkm();
    STAMP(1); CSTAMP(10);
    rd(smem, 0, xa, wa);
#define ILV() do { _Pragma("unroll") for (int q_ = 0; q_ < 13; ++q_) { __builtin_amdgcn_sched_group_barrier(0x8, 1, 0); __builtin_amdgcn_sched_group_barrier(0x100, 1, 0); } \
                   __builtin_amdgcn_sched_group_barrier(0x8, 7, 0); } while (0)
    K1UNROLL
    for (int j = 0; j < 9; ++j) {
      rd(smem + (j & 3) * K1_STAGE, 1, xb, wb);
      mm(xa, wa);
      ILV();
      __builtin_amdgcn_sched_barrier(0);
      bar_lgkm();
      rd(smem + ((j + 1) & 3) * K1_STAGE, 0, xa, wa);
      mm(xb, wb);
      ILV();
      __builtin_amdgcn_sched_barrier(0);
    }
    STAMP(2); CSTAMP(11);
    char* st = smem + wave * (80 * ST_LD);
#pragma unroll
    for (int m = 0; m < 5; ++m)
#pragma unroll
      for (int n = 0; n < 4; ++n) {
        u32x2 u = {pk2(acc[m][n][0], acc[m][n][1]), pk2(acc[m][n][2], acc[m][n][3])};
        *(u32x2*)(st + (m * 16 + fr) * ST_LD + n * 32 + kg * 8) = u;
      }
    asm volatile("s_waitcnt lgkmcnt(0)" ::: "memory");
    STAMP(3);
    if (__builtin_expect(!(last && wn == 1), 1)) {
      const int ld = isA ? 3 * ROWB : ROWB, r0 = row0 + wm * 80 + (lane >> 3);
      char* d = (isA ? ws + WS_QKV : ws + WS_QB) + (size_t)r0 * ld + ct * 256 + wn * 128 + (lane & 7) * 16;
      u32x4 v[10];
#pragma unroll
      for (int i = 0; i < 10; ++i) v[i] = *(const u32x4*)(st + (i * 8 + (lane >> 3)) * ST_LD + (lane & 7) * 16);
#pragma unroll
      for (int i = 0; i < 10; ++i) if (r0 + i * 8 < SEQ) ST16K(d + (size_t)i * 8 * ld, v[i]);
    }
  }
  if (__builtin_expect(!isA && last, 0)) {
    bar_lgkm();
    const char* st = smem + 2 * (80 * ST_LD);
    char* kv = ws + WS_KV;
    if (tid < 320) {
      const int r = tid >> 1, hh = tid & 1, key = row0 + r;
      if (key < SEQ) {
        const int t = key >> 5, k32 = key & 31;
        char* g = kv + (t >> 2) * GRP_B;
        u32x4 kx = *(const u32x4*)(st + r * ST_LD + hh * 16);
#pragma unroll
        for (int e = 0; e < 4; ++e) { const unsigned u = kx[e]; kx[e] = pk2(bf_lo(u) * QSCALE, bf_hi(u) * QSCALE); }
        *(u32x4*)(g + (t & 3) * 1024 + (hh * 32 + k32) * 16) = kx;
        const unsigned kr = *(const unsigned*)(st + r * ST_LD + 32);
        if (hh == 0) *(unsigned*)(g + KR_OFF + k32 * 16 + (t & 3) * 4) = pk2(bf_lo(kr) * QSCALE, bf_hi(kr) * QSCALE);
      }
    }
    const int t2 = 511 - tid;
    if (t2 < 400) {
      const int blk = t2 / 20, d = t2 - blk * 20;
      const int tl = blk >> 2, s = (blk >> 1) & 1, hh = blk & 1;
      const int t = (row0 >> 5) + tl;
      if (t < 64) {
        const char* src = st + (tl * 32 + s * 16 + hh * 4) * ST_LD + 64 + (d < 18 ? d : 0) * 2;
        u32x4 o;
#pragma unroll
        for (int e = 0; e < 4; ++e) {
          const int r = (e >> 1) * 8 + (e & 1) * 2;
          o[e] = (unsigned)*(const unsigned short*)(src + r * ST_LD) | ((unsigned)*(const unsigned short*)(src + (r + 1) * ST_LD) << 16);
          o[e] = d < 18 ? o[e] : (d == 18 ? 0x3f803f80u : 0u);
        }
        *(u32x4*)(kv + (t >> 2) * GRP_B + V_OFF + (t & 3) * VT_B + (s * 2 + hh) * 320 + d * 16) = o;
      }
    }
  }
  STAMP(9);
  STAMP_OUT(0, ws);
}

constexpr int K2_LDS = KV_BYTES + 6144;
constexpr float M_OFF = 96.0f;
#define SB __builtin_amdgcn_sched_barrier(0)
#define MF16(a, b, c) __builtin_amdgcn_mfma_f32_32x32x16_bf16(a, b, c, 0, 0, 0)
#define MF8(a, b, c) __builtin_amdgcn_mfma_f32_32x32x8bf16_1k(a, b, c, 0, 0, 0)
#define EX(i) cur[i] = __builtin_amdgcn_exp2f(cur[i])

DI float xhalf(float x);
DI float max16(const f32x16& s, float m) {
#pragma unroll
  for (int i = 0; i < 16; i += 2) m = __builtin_fmaxf(__builtin_fmaxf(s[i], s[i + 1]), m);
  return m;
}
#define E(i) EX(i); SB;
#define C0(i) p0[i] = pk2(cur[2 * i], cur[2 * i + 1]); SB;
#define C1(i) p1[i] = pk2(cur[8 + 2 * i], cur[9 + 2 * i]); SB;
template <bool RELOAD>
DI void phase(f32x16& cur, f32x16& nxt, s16x4& kra, const char* krp, const s16x4 qr, bf16x8& kf, const char* kp2, const bf16x8 qf,
              bf16x8& v0, const char* v0p, bf16x8& v1, const char* v1p, u32x4& p0, u32x4& p1, f32x16& o) {
  const f32x16 z = {};
  nxt = MF8(kra, qr, z); SB;
  if (RELOAD) { kra = *(const s16x4*)krp; SB; }
  E(0) E(1) E(2) E(3) C0(0) E(4)
  o = MF16(v1, __builtin_bit_cast(bf16x8, p1), o); SB;
  v1 = *(const bf16x8*)v1p; SB;
  C0(1) E(5) E(6) E(7) C0(2) E(8)
  nxt = MF16(kf, qf, nxt); SB;
  kf = *(const bf16x8*)kp2; SB;
  C0(3) E(9) E(10) E(11) C1(0) E(12)
  o = MF16(v0, __builtin_bit_cast(bf16x8, p0), o); SB;
  v0 = *(const bf16x8*)v0p; SB;
  C1(1) E(13) E(14) E(15) C1(2) C1(3)
}
DI unsigned rem_word(unsigned qrem, float m, int h) {
  const unsigned hi = pk2(-m, 0.f) & 0xffffu;
  const unsigned lo = pk2(-m - bf_lo(hi), 0.f) & 0xffffu;
  return h ? (hi | (lo << 16)) : qrem;
}
DI s16x4 mk2(unsigned a, unsigned b) { u32x2 r = {a, b}; return __builtin_bit_cast(s16x4, r); }

DI float max_pass(const char* kp, const char* krp, const bf16x8 qf, unsigned rw, int npair) {
  const s16x4 qe = mk2(rw, 0u), qo = mk2(0u, rw);
  const f32x16 z = {};
  float m = -3.0e38f;
  for (int i = 0; i < npair; ++i) {
    const int G = (i >> 1) * GRP_B, hf = i & 1;
    const s16x4 kr = *(const s16x4*)(krp + G + hf * 8);
    m = max16(MF16(*(const bf16x8*)(kp + G + hf * 2048), qf, MF8(kr, qe, z)), m);
    m = max16(MF16(*(const bf16x8*)(kp + G + hf * 2048 + 1024), qf, MF8(kr, qo, z)), m);
  }
  return fmaxf(m, xhalf(m));
}
DI void attn_exact(const char* img, int ko, int kro, int vo, const bf16x8 qf, unsigned qrem, int h, f32x16& o) {
  const float mt = max_pass(img + ko, img + kro, qf, rem_word(qrem, 0.f, h), 32);
  const unsigned rw = rem_word(qrem, mt, h);
  const s16x4 qe = mk2(rw, 0u), qo = mk2(0u, rw);
  const f32x16 z = {};
  o = z;
  for (int t = 0; t < 64; ++t) {
    const char* g = img + (t >> 2) * GRP_B;
    const s16x4 kr = *(const s16x4*)(g + kro + (t & 2) * 4);
    f32x16 cur = MF16(*(const bf16x8*)(g + ko + (t & 3) * 1024), qf, MF8(kr, (t & 1) ? qo : qe, z));
    u32x4 p0, p1;
#pragma unroll
    for (int i = 0; i < 16; ++i) EX(i);
#pragma unroll
    for (int i = 0; i < 4; ++i) { p0[i] = pk2(cur[2 * i], cur[2 * i + 1]); p1[i] = pk2(cur[8 + 2 * i], cur[9 + 2 * i]); }
    o = MF16(*(const bf16x8*)(g + vo + (t & 3) * VT_B), __builtin_bit_cast(bf16x8, p0), o);
    o = MF16(*(const bf16x8*)(g + vo + (t & 3) * VT_B + 640), __builtin_bit_cast(bf16x8, p1), o);
  }
}

constexpr int LA_0 = 98304;
constexpr int LA_Q = LA_0 + 22 * 2304;
constexpr int L_OB = 94208;
constexpr int L_P = 104448;
constexpr int P_LD = 272, P_HALF = 80 * P_LD;
constexpr int XC_B = 10240;
constexpr int L_CTL = K2_LDS;
constexpr int K2_LDS_ALL = K2_LDS + 64;
enum { C_KVA = 0, C_KVB = 4, C_PAST = 8, C_AROWS = 12, C_A2 = 16, C_XA = 20, C_DONE = 24, C_PROG = 32 };

DI void lds_add(unsigned addr) {
  asm volatile("s_waitcnt lgkmcnt(0)\n\ts_mov_b64 exec, 1\n\tds_add_u32 %0, %1\n\ts_mov_b64 exec, -1" ::"v"(addr), "v"(1u) : "memory");
}
template <int N> DI void lds_wait(unsigned addr) {
  unsigned r, t;
  asm volatile("1:\n\tds_read_b32 %0, %2\n\ts_waitcnt lgkmcnt(0)\n\tv_readfirstlane_b32 %1, %0\n\ts_cmp_lt_u32 %1, %3\n\ts_cbranch_scc1 1b"
               : "=&v"(r), "=&s"(t) : "v"(addr), "i"(N) : "memory", "scc");
}
DI void cnt_wait(const char* p) {
  unsigned r, n;
  asm volatile("s_mov_b32 %1, 0x400000\n1:\n\tglobal_load_dword %0, %2, off sc1\n\ts_sub_u32 %1, %1, 1\n\ts_cbranch_scc1 2f\n\t"
               "s_waitcnt vmcnt(0)\n\tv_cmp_gt_u32 vcc, 16, %0\n\ts_cbranch_vccz 2f\n\ts_sleep " POLL_SLEEP "\n\ts_branch 1b\n2:\n\ts_waitcnt vmcnt(0)"
               : "=&v"(r), "=&s"(n) : "v"(p) : "memory", "scc", "vcc");
}
DI float xhalf(float x) {
  const unsigned u = __float_as_uint(x);
  const auto r = __builtin_amdgcn_permlane32_swap(u, u, false, false);
  return __uint_as_float((threadIdx.x & 32) ? r[0] : r[1]);
}
DI void local_attn(char* smem, unsigned lds0, int wave, int c32, int h, int row) {
  unsigned* qp = (unsigned*)(smem + LA_Q + wave * ROWB + c32 * 36);
  unsigned q[9];
#pragma unroll
  for (int e = 0; e < 9; ++e) q[e] = qp[e];
  const unsigned kb = lds0 + LA_0 + (wave + 8 - h * 8) * 2304 + c32 * 36;
  const unsigned vb = lds0 + LA_0 + wave * 2304 + ROWB + c32 * 36 + h * 16;
  u32x2 kk[9][2], vv[5][4];
#pragma unroll
  for (int e = 0; e < 9; ++e)
    asm volatile("ds_read2st64_b32 %0, %2 offset0:54 offset1:36\n\tds_read2st64_b32 %1, %2 offset0:18 offset1:0"
                 : "=&v"(kk[e][0]), "=&v"(kk[e][1]) : "v"(kb + e * 4) : "memory");
  asm volatile("s_waitcnt lgkmcnt(0)" : "+v"(kk[0][0]), "+v"(kk[0][1]), "+v"(kk[1][0]), "+v"(kk[1][1]), "+v"(kk[2][0]), "+v"(kk[2][1]),
      "+v"(kk[3][0]), "+v"(kk[3][1]), "+v"(kk[4][0]), "+v"(kk[4][1]), "+v"(kk[5][0]), "+v"(kk[5][1]), "+v"(kk[6][0]), "+v"(kk[6][1]),
      "+v"(kk[7][0]), "+v"(kk[7][1]), "+v"(kk[8][0]), "+v"(kk[8][1])::"memory");
#pragma unroll
  for (int e = 0; e < 5; ++e)
    asm volatile("ds_read2st64_b32 %0, %4 offset0:126 offset1:108\n\tds_read2st64_b32 %1, %4 offset0:90 offset1:72\n\t"
                 "ds_read2st64_b32 %2, %4 offset0:54 offset1:36\n\tds_read2st64_b32 %3, %4 offset0:18 offset1:0"
                 : "=&v"(vv[e][0]), "=&v"(vv[e][1]), "=&v"(vv[e][2]), "=&v"(vv[e][3]) : "v"(vb + e * 4) : "memory");
  float lg[4] = {0.f, 0.f, 0.f, 0.f};
#pragma unroll
  for (int e = 0; e < 9; ++e) {
    const unsigned k0 = kk[e][0][0], k1 = kk[e][0][1], k2 = kk[e][1][0], k3 = kk[e][1][1];
    lg[0] = dot2(q[e], k0, lg[0]); lg[1] = dot2(q[e], k1, lg[1]); lg[2] = dot2(q[e], k2, lg[2]); lg[3] = dot2(q[e], k3, lg[3]);
  }
#pragma unroll
  for (int w = 0; w < 4; ++w) lg[w] = row - 2 * (h * 4 + w) >= 0 ? lg[w] * QSCALE : -1.0e9f * LOG2E;
  float mx = fmaxf(fmaxf(lg[0], lg[1]), fmaxf(lg[2], lg[3]));
  mx = fmaxf(mx, xhalf(mx));
  float sum = 0.f;
#pragma unroll
  for (int w = 0; w < 4; ++w) { lg[w] = __builtin_amdgcn_exp2f(lg[w] - mx); sum += lg[w]; }
  sum += xhalf(sum);
  const float inv = __builtin_amdgcn_rcpf(sum);
  const unsigned pa = pk2(lg[0] * inv, lg[1] * inv), pb = pk2(lg[2] * inv, lg[3] * inv);
  const auto ra = __builtin_amdgcn_permlane32_swap(pa, pa, false, false);
  const auto rb = __builtin_amdgcn_permlane32_swap(pb, pb, false, false);
  const unsigned pp[4] = {ra[0], rb[0], ra[1], rb[1]};
  asm volatile("s_waitcnt lgkmcnt(0)" : "+v"(vv[0][0]), "+v"(vv[0][1]), "+v"(vv[0][2]), "+v"(vv[0][3]), "+v"(vv[1][0]), "+v"(vv[1][1]),
      "+v"(vv[1][2]), "+v"(vv[1][3]), "+v"(vv[2][0]), "+v"(vv[2][1]), "+v"(vv[2][2]), "+v"(vv[2][3]), "+v"(vv[3][0]), "+v"(vv[3][1]),
      "+v"(vv[3][2]), "+v"(vv[3][3]), "+v"(vv[4][0]), "+v"(vv[4][1]), "+v"(vv[4][2]), "+v"(vv[4][3])::"memory");
#pragma unroll
  for (int e = 0; e < 5; ++e) {
    float o0 = 0.f, o1 = 0.f;
#pragma unroll
    for (int w2 = 0; w2 < 4; ++w2) {
      const unsigned x = vv[e][w2][0], y = vv[e][w2][1];
      o0 = dot2(__builtin_amdgcn_perm(y, x, 0x05040100u), pp[w2], o0);
      o1 = dot2(__builtin_amdgcn_perm(y, x, 0x07060302u), pp[w2], o1);
    }
    qp[h * 4 + e] = pk2(o0, o1);
  }
}
template <int MASK> DI void glds16_part(unsigned voff, const void* sbase, unsigned lds_dst) {
  unsigned keep;
  asm volatile("s_mov_b32 %0, m0\n\ts_mov_b32 m0, %3\n\ts_mov_b64 exec, %4\n\tglobal_load_lds_dwordx4 %1, %2\n\ts_mov_b64 exec, -1\n\ts_mov_b32 m0, %0"
               : "=&s"(keep) : "v"(voff), "s"(sbase), "s"(lds_dst), "i"(MASK) : "memory");
}
DI void glds16_sc1(unsigned voff, const void* sbase, unsigned lds_dst) {
  unsigned keep;
  asm volatile("s_mov_b32 %0, m0\n\ts_mov_b32 m0, %3\n\ts_nop 0\n\tglobal_load_lds_dwordx4 %1, %2 sc1\n\ts_mov_b32 m0, %0"
               : "=&s"(keep) : "v"(voff), "s"(sbase), "s"(lds_dst) : "memory");
}
DI void store_row(const char* stage, char* dst, int lane) {
  asm volatile("s_waitcnt lgkmcnt(0)" ::: "memory");
  const u32x4 a = *(const u32x4*)(stage + lane * 16);
  const u32x4 b = *(const u32x4*)(stage + 1024 + (lane & 7) * 16);
  asm volatile("global_store_dwordx4 %0, %1, off sc1\n\ts_nop 1" ::"v"(dst + lane * 16), "v"(a) : "memory");
  if (lane < 8) asm volatile("global_store_dwordx4 %0, %1, off offset:1024 sc1\n\ts_nop 1" ::"v"(dst + lane * 16), "v"(b) : "memory");
}
DI void signal_row(char* cnt, int lane) {
  asm volatile("s_waitcnt vmcnt(0)" ::: "memory");
  if (lane == 0) __hip_atomic_fetch_add((unsigned*)cnt, 1u, __ATOMIC_RELAXED, __HIP_MEMORY_SCOPE_AGENT);
}

__global__ __launch_bounds__(512) void k2_attn(char* __restrict__ ws, char* __restrict__ out) {
  extern __shared__ __attribute__((aligned(16))) char smem[];
  asm volatile("" ::"s"(ws), "s"(out));
  const int tid = threadIdx.x, lane = tid & 63;
  const int wave = __builtin_amdgcn_readfirstlane(tid >> 6);
  STAMP_DECL; STAMP(0);
  const int b = blockIdx.x;
  const int rblk = (b & 7) * 32 + (b >> 3);
  const int row = rblk * 8 + wave;
  const int c32 = lane & 31, h = lane >> 5;
  const unsigned lds0 = (unsigned)(uintptr_t)smem;
  const unsigned ctl = lds0 + L_CTL;
  const int ko = lane * 16;
  auto kv_fetch = [&](int p0, int step, int n) {
#pragma unroll
    for (int i = 0; i < n; ++i) glds16(ko, ws + WS_KV + (p0 + step * i) * 1024, lds0 + (p0 + step * i) * 1024);
  };
  bf16x8 qf;
  unsigned qrem;
  {
    const char* qp = ws + WS_QB + (size_t)row * ROWB + c32 * 36;
    asm volatile("global_load_dwordx4 %0, %1, off" : "=v"(qf) : "v"(qp + h * 16) : "memory");
    asm volatile("global_load_dword %0, %1, off offset:32" : "=v"(qrem) : "v"(qp) : "memory");
    kv_fetch(wave, 8, 5);
  }
  if (tid < 16) *(unsigned*)(smem + L_CTL + tid * 4) = 0u;
  const int vrow = c32 < 19 ? c32 : (c32 == 28 ? 18 : 19);
  const int kro = h ? V_OFF + 18 * 16 : KR_OFF + c32 * 16;
  const int vo = V_OFF + h * 320 + vrow * 16;
  const int rg = rblk / 9, cg = rblk - rg * 9;
  const int nrt = rg < 16 ? 5 : 4, rt0 = rg < 16 ? rg * 5 : 16 + rg * 4;
  const bool has_tile = rblk < 252;
  const int fr = lane & 15, kg = lane >> 4;
  const unsigned xo = (lane >> 3) * ROWB + (((lane & 7) ^ (lane >> 3)) << 4);
  auto fetch_tile = [&](const char* src) {
    const char* s0 = src + (size_t)(rt0 * 16 + wave * 8) * ROWB;
    const char* s1 = src + (size_t)(rt0 * 16 + 64 + (wave & 1) * 8) * ROWB;
    if (nrt == 5) {
#pragma unroll
      for (int c = 0; c < 9; ++c) {
        glds16_sc1(xo, s0 + c * 128, lds0 + c * XC_B + wave * 1024);
        if (c % 3 == 2) {
          const int ce = c == 8 ? 8 : (c / 3) * 4 + (wave >> 1);
          glds16_sc1(xo, s1 + ce * 128, lds0 + ce * XC_B + (8 + (wave & 1)) * 1024);
        }
      }
    } else {
#pragma unroll
      for (int c = 0; c < 9; ++c) glds16_sc1(xo, s0 + c * 128, lds0 + c * XC_B + wave * 1024);
    }
  };
  const char* cnt_p = ws + WS_CNT + ((has_tile ? rt0 : 0) + (lane < nrt ? lane : 0)) * 128;
  const char* wfa = ws + WS_WF + (size_t)((has_tile ? cg * 4 : 0) + (wave & 3)) * 18432 + (wave >> 2) * 1024;
  const f32x16 z = {};
  f32x16 o = z;
  u32x4 wA[9], wB[9];
  STAMP(1);
  asm volatile("s_waitcnt vmcnt(0)" : "+v"(qf), "+v"(qrem)::"memory");
  bar_lgkm();
  STAMP(2);
  if (wave >= 4) {
    const int w4 = wave - 4;
    kv_fetch(40 + w4, 4, 14);
#pragma unroll
    for (int i = 0; i < 6; ++i) {
      int r = w4 + 4 * i;
      r = r > 21 ? 21 : r;
      const int gr = rblk * 8 + r - 14;
      const char* src = ws + WS_QKV + (size_t)(gr < 0 ? 0 : gr) * (3 * ROWB) + ROWB;
      glds16(ko, src, lds0 + LA_0 + r * 2304);
      glds16(ko, src + 1024, lds0 + LA_0 + r * 2304 + 1024);
      glds16_part<0xffff>(ko, src + 2048, lds0 + LA_0 + r * 2304 + 2048);
    }
#pragma unroll
    for (int i = 0; i < 2; ++i) {
      const char* src = ws + WS_QKV + (size_t)(rblk * 8 + w4 + 4 * i) * (3 * ROWB);
      glds16(ko, src, lds0 + LA_Q + (w4 + 4 * i) * ROWB);
      glds16_part<0xff>(ko, src + 1024, lds0 + LA_Q + (w4 + 4 * i) * ROWB + 1024);
    }
    wait_vm<22>();
    lds_add(ctl + C_KVA);
  }
  const float ms = max_pass(smem + ko, smem + kro, qf, rem_word(qrem, 0.f, h), 1);
  const unsigned rw = rem_word(qrem, bf_lo(pk2(ms + M_OFF, 0.f)), h);
  {
    int kq = ko, krq = kro, vq = vo;
    const char *kp = smem + kq, *krp = smem + krq, *vp = smem + vq;
    const s16x4 qe = mk2(rw, 0u), qo = mk2(0u, rw);
    s16x4 k01 = *(const s16x4*)krp, k23 = *(const s16x4*)(krp + 8);
    f32x16 sa = MF16(*(const bf16x8*)kp, qf, MF8(k01, qe, z)), sb;
    bf16x8 kf = *(const bf16x8*)(kp + 1024), v0 = *(const bf16x8*)vp, v1 = v0;
    u32x4 p0 = {}, p1 = {};
    unsigned cnt_seen;
    STAMP(3); CSTAMP(10);
#define WF9(W, BASE) asm volatile( \
        "global_load_dwordx4 %0, %9, %10\n\tglobal_load_dwordx4 %1, %9, %10 offset:2048\n\t" \
        "global_load_dwordx4 %2, %11, %10 offset:-4096\n\tglobal_load_dwordx4 %3, %11, %10 offset:-2048\n\t" \
        "global_load_dwordx4 %4, %11, %10\n\tglobal_load_dwordx4 %5, %11, %10 offset:2048\n\t" \
        "global_load_dwordx4 %6, %12, %10 offset:-4096\n\tglobal_load_dwordx4 %7, %12, %10 offset:-2048\n\t" \
        "global_load_dwordx4 %8, %12, %10" \
        : "=&v"(W[0]), "=&v"(W[1]), "=&v"(W[2]), "=&v"(W[3]), "=&v"(W[4]), "=&v"(W[5]), "=&v"(W[6]), "=&v"(W[7]), "=&v"(W[8]) \
        : "v"(ko), "s"(BASE), "v"(ko + 8192), "v"(ko + 16384) : "memory")
    auto group = [&](auto gc) __attribute__((always_inline)) {
      constexpr int g = decltype(gc)::value;
      LSTAMP(2 * g);
      if constexpr (PRIO(g) != PRIO(g - 1)) __builtin_amdgcn_s_setprio(PRIO(g));
      if constexpr (g == 1) { wait_vm<0>(); lds_add(ctl + C_AROWS); }
      if constexpr (g == 3) lds_wait<4>(ctl + C_KVA);
      if constexpr (g == 3 || g == 4) {
        if ((wave >> 2) == 4 - g) {
          lds_wait<8>(ctl + C_AROWS);
          local_attn(smem, lds0, wave, c32, h, row);
          store_row(smem + LA_Q + wave * ROWB, ws + WS_OA + (size_t)row * ROWB, lane);
          lds_add(ctl + C_A2);
        }
      }
      if constexpr (g == 6) {
        signal_row(ws + WS_CNT + (row >> 4) * 128, lane);
        lds_wait<8>(ctl + C_A2);
        kv_fetch(96 + wave, 8, 7);
      }
      if constexpr (g == 7) { wait_vm<0>(); lds_add(ctl + C_KVB); }
      if constexpr (g == 8) WF9(wA, wfa);
      if constexpr (g == 9) lds_wait<8>(ctl + C_KVB);
      if constexpr (g == 10) WF9(wB, wfa + 36 * 18432);
      if constexpr (g == 11) lds_add(ctl + C_PAST);
      if constexpr (g == 12) asm volatile("global_load_dword %0, %1, off sc1" : "=v"(cnt_seen) : "v"(cnt_p) : "memory");
      if constexpr (g == 13) {
        lds_wait<8>(ctl + C_PAST);
        asm volatile("s_waitcnt vmcnt(0)" : "+v"(cnt_seen)::"memory");
        if (has_tile) {
          if (!__all(cnt_seen >= 16u)) cnt_wait(cnt_p);
          fetch_tile(ws + WS_OA);
        }
      }
      if constexpr (g == 15) { wait_vm<0>(); lds_add(ctl + C_XA); }
      LSTAMP(2 * g + 1);
      SB;
      const int G = (g & 3) * GRP_B;
      phase<true>(sa, sb, k01, krp + G + GRP_B, qo, kf, kp + G + 2048, qf, v0, vp + G + VT_B, v1, vp + G + 640, p0, p1, o);
      phase<false>(sb, sa, k23, nullptr, qe, kf, kp + G + 3072, qf, v0, vp + G + 2 * VT_B, v1, vp + G + VT_B + 640, p0, p1, o);
      phase<true>(sa, sb, k23, krp + G + GRP_B + 8, qo, kf, kp + G + GRP_B, qf, v0, vp + G + 3 * VT_B, v1, vp + G + 2 * VT_B + 640, p0, p1, o);
      phase<false>(sb, sa, k01, nullptr, qe, kf, kp + G + GRP_B + 1024, qf, v0, vp + G + GRP_B, v1, vp + G + 3 * VT_B + 640, p0, p1, o);
      asm volatile("" : "+v"(o), "+v"(sa), "+v"(p1));
      if constexpr ((g & 3) == 3) {
        kq += 4 * GRP_B; krq += 4 * GRP_B; vq += 4 * GRP_B;
        asm volatile("" : "+v"(kq), "+v"(krq), "+v"(vq));
        kp = smem + kq; krp = smem + krq; vp = smem + vq;
      }
    };
#define GR(i) group(IC<i>{});
    GR(0) GR(1) GR(2) GR(3) GR(4) GR(5) GR(6) GR(7) GR(8) GR(9) GR(10) GR(11) GR(12) GR(13) GR(14) GR(15)
    o = MF16(v1, __builtin_bit_cast(bf16x8, p1), o);
  }
  STAMP(4); CSTAMP(11); LSTAMP(32);
  lds_add(ctl + C_DONE);
  float l = h ? o[12] : o[10];
  float big = l;
#pragma unroll
  for (int i = 0; i < 10; ++i) big += fabsf(o[i]);
  const bool ok = big < 1.0e37f && l > 0.f;
  if (__builtin_expect(!__all(ok), 0)) {
    attn_exact(ws + WS_KV, ko, kro, vo, qf, qrem, h, o);
    l = h ? o[12] : o[10];
  }
  {
    const float inv = __builtin_amdgcn_rcpf(l);
    unsigned* dst = (unsigned*)(smem + L_OB + wave * ROWB + c32 * 36 + h * 8);
    dst[0] = pk2(o[0] * inv, o[1] * inv);
    dst[1] = pk2(o[2] * inv, o[3] * inv);
    dst[4] = pk2(o[4] * inv, o[5] * inv);
    dst[5] = pk2(o[6] * inv, o[7] * inv);
    if (!h) dst[8] = pk2(o[8] * inv, o[9] * inv);
    store_row(smem + L_OB + wave * ROWB, ws + WS_OB + (size_t)row * ROWB, lane);
  }
  char* cntB = ws + WS_CNT + (128 + (row >> 4)) * 128;
  if (!has_tile) { signal_row(cntB, lane); return; }
  const int xa = fr * 128 + ((((wave >> 2) * 4 + kg) ^ (fr & 7)) << 4);
  char* pw = smem + L_P + (wave >> 2) * P_HALF + fr * P_LD + (wave & 3) * 64 + kg * 16;
  f32x4 acc[5];
  auto mm3 = [&](u32x4 (&w)[9], int c0) {
    bf16x8 xf[3][5];
#pragma unroll
    for (int c = 0; c < 3; ++c)
#pragma unroll
      for (int m = 0; m < 5; ++m) xf[c][m] = *(const bf16x8*)(smem + (c0 + c) * XC_B + xa + m * 2048);
    SB;
#pragma unroll
    for (int c = 0; c < 3; ++c)
#pragma unroll
      for (int m = 0; m < 5; ++m) acc[m] = MFMA16(__builtin_bit_cast(bf16x8, w[c0 + c]), xf[c][m], (c0 + c) ? acc[m] : f32x4{});
  };
  auto put = [&] {
#pragma unroll
    for (int m = 0; m < 5; ++m) *(f32x4*)(pw + m * 16 * P_LD) = acc[m];
  };
  auto tile_out = [&](int br) {
#pragma unroll
    for (int i = 0; i < 2; ++i) {
      const int r = (tid >> 3) + 64 * i, c = tid & 7;
      if (r < nrt * 16) {
        const char* p = smem + L_P + r * P_LD + c * 32;
        const f32x4 a0 = *(const f32x4*)p, a1 = *(const f32x4*)(p + 16);
        const f32x4 b0 = *(const f32x4*)(p + P_HALF), b1 = *(const f32x4*)(p + P_HALF + 16);
        const u32x4 u = {pk2(a0[0] + b0[0], a0[1] + b0[1]), pk2(a0[2] + b0[2], a0[3] + b0[3]),
                         pk2(a1[0] + b1[0], a1[1] + b1[1]), pk2(a1[2] + b1[2], a1[3] + b1[3])};
        OUTST(out + (size_t)(rt0 * 16 + r) * (2 * ROWB) + br * ROWB + cg * 128 + c * 16, u);
      }
    }
  };
  signal_row(cntB, lane);
  STAMP(5); LSTAMP(33);
  lds_wait<8>(ctl + C_XA);
  mm3(wA, 0);
  mm3(wA, 3);
  mm3(wA, 6);
  LSTAMP(34);
  lds_wait<8>(ctl + C_DONE);
  put();
  LSTAMP(35);
  if (wave == 0) cnt_wait(cnt_p + 128 * 128);
  bar_lgkm();
  STAMP(6); LSTAMP(36);
  fetch_tile(ws + WS_OB);
  LSTAMP(37);
  tile_out(0);
  LSTAMP(38);
  if (nrt == 5) wait_vm<8>(); else wait_vm<6>();
  bar_lgkm();
  STAMP(7); LSTAMP(39);
  mm3(wB, 0);
  if (nrt == 5) wait_vm<4>(); else wait_vm<3>();
  bar_lgkm();
  mm3(wB, 3);
  wait_vm<0>();
  bar_lgkm();
  mm3(wB, 6);
  put();
  bar_lgkm();
  STAMP(8);
  tile_out(1);
  STAMP(9);
  STAMP_OUT(1, ws); LSTAMP_OUT(ws);
}

extern "C" void kernel_launch(void* const* d_in, const int* in_sizes, int n_in, void* d_out, int out_size,
                              void* d_ws, size_t ws_size, hipStream_t stream) {
  (void)in_sizes; (void)n_in; (void)out_size; (void)ws_size;
  (void)hipFuncSetAttribute((const void*)k1_proj, hipFuncAttributeMaxDynamicSharedMemorySize, K1_LDS);
  (void)hipFuncSetAttribute((const void*)k2_attn, hipFuncAttributeMaxDynamicSharedMemorySize, K2_LDS_ALL);
  char* ws = (char*)d_ws;
  k1_proj<<<256, 512, K1_LDS, stream>>>((const char*)d_in[0], (const char*)d_in[1], (const char*)d_in[3], (const char*)d_in[4],
                                        (const char*)d_in[5], (const char*)d_in[6], (const char*)d_in[2], (const char*)d_in[7], ws);
  k2_attn<<<256, 512, K2_LDS_ALL, stream>>>(ws, (char*)d_out);
}
```

```cpp
#include <hip/hip_runtime.h>
#include <hip/hip_bf16.h>
#include <stdint.h>

typedef __bf16 bf16x2 __attribute__((ext_vector_type(2)));
typedef __bf16 bf16x8 __attribute__((ext_vector_type(8)));
typedef short s16x4 __attribute__((ext_vector_type(4)));
typedef float f32x2 __attribute__((ext_vector_type(2)));
typedef float f32x4 __attribute__((ext_vector_type(4)));
typedef float f32x16 __attribute__((ext_vector_type(16)));
typedef unsigned u32x2 __attribute__((ext_vector_type(2)));
typedef unsigned u32x4 __attribute__((ext_vector_type(4)));
#define LDSP(T) __attribute__((address_space(3))) T*
#define DI __device__ __forceinline__

#define K1UNROLL _Pragma("unroll")
#define PRIO(g) ((g) < 0 ? -1 : (g) < 8 ? 3 - ((g) >> 1) : (g) < 11 ? 3 : (g) < 13 ? 2 : (g) < 15 ? 1 : 0)
#define POLL_SLEEP "4"
#define SIGC 3
constexpr int SEQ = 2048, DM = 576;
constexpr int ROWB = DM * 2;
constexpr float LOG2E = 1.4426950408889634f;
constexpr float QSCALE = 0.23570226039551584f * LOG2E;

constexpr int GRP_B = 9728, KR_OFF = 4096, V_OFF = 4608, VT_B = 1280;
constexpr int KV_BYTES = 16 * GRP_B;
constexpr size_t WS_KV = 0;
constexpr size_t WS_QB = WS_KV + KV_BYTES;
constexpr size_t WS_QKV = WS_QB + (size_t)SEQ * ROWB;
constexpr size_t WS_OA = WS_QKV + (size_t)SEQ * 3 * ROWB;
constexpr size_t WS_OB = WS_OA + (size_t)SEQ * ROWB;
constexpr size_t WS_WF = WS_OB + (size_t)SEQ * ROWB;
constexpr size_t WS_CNT = WS_WF + (size_t)72 * 18432;

template <int N> struct IC { static constexpr int value = N; };
DI unsigned pk2(float a, float b) {
  f32x2 f = {a, b};
  return __builtin_bit_cast(unsigned, __builtin_convertvector(f, bf16x2));
}
DI float dot2(unsigned a, unsigned b, float c) {
  return __builtin_amdgcn_fdot2_f32_bf16(__builtin_bit_cast(bf16x2, a), __builtin_bit_cast(bf16x2, b), c, false);
}
DI float bf_lo(unsigned u) { return __uint_as_float(u << 16); }
DI float bf_hi(unsigned u) { return __uint_as_float(u & 0xffff0000u); }

DI void glds16(unsigned voff, const void* sbase, unsigned lds_dst) {
  unsigned keep;
  asm volatile("s_mov_b32 %0, m0\n\ts_mov_b32 m0, %3\n\ts_nop 0\n\tglobal_load_lds_dwordx4 %1, %2\n\ts_mov_b32 m0, %0"
               : "=&s"(keep) : "v"(voff), "s"(sbase), "s"(lds_dst) : "memory");
}
DI void glds4(unsigned voff, const void* sbase, unsigned lds_dst) {
  unsigned keep;
  asm volatile("s_mov_b32 %0, m0\n\ts_mov_b32 m0, %3\n\ts_nop 0\n\tglobal_load_lds_dword %1, %2\n\ts_mov_b32 m0, %0"
               : "=&s"(keep) : "v"(voff), "s"(sbase), "s"(lds_dst) : "memory");
}
template <int N> DI void wait_vm() { asm volatile("s_waitcnt vmcnt(%0)" ::"i"(N) : "memory"); }
DI void bar_lgkm() { asm volatile("s_waitcnt lgkmcnt(0)\n\ts_barrier" ::: "memory"); }
DI bf16x8 tr_frag(const char* p) {
  s16x4 lo = __builtin_amdgcn_ds_read_tr16_b64_v4i16((LDSP(s16x4))(p));
  s16x4 hi = __builtin_amdgcn_ds_read_tr16_b64_v4i16((LDSP(s16x4))(p + 256));
  return __builtin_bit_cast(bf16x8, __builtin_shufflevector(lo, hi, 0, 1, 2, 3, 4, 5, 6, 7));
}
#define ST16(p, v) __builtin_nontemporal_store(v, (u32x4*)(p))
DI void ST16K(char* p, u32x4 v) { asm volatile("global_store_dwordx4 %0, %1, off sc1\n\ts_nop 1" :: "v"(p), "v"(v) : "memory"); }
#define OUTST ST16K
#define MFMA16(a, b, c) __builtin_amdgcn_mfma_f32_16x16x32_bf16(a, b, c, 0, 0, 0)

constexpr int K1_XB = 20480, K1_STAGE = 36864, K1_LDS = 4 * K1_STAGE;
constexpr int K1_RB = 13, K1_ACT = 14, K1_BCT = 5, K1_GEMM = K1_RB * (K1_ACT + K1_BCT);

template <int MODE>
DI void k1_loader(const char* X, int row0, const char* W0, const char* W1, int ldw, int colB0, int colB1,
                  const char* wk, const char* wv, int rot, unsigned lds0, int l, int lane) {
  unsigned xo[5], wo[4], eo[8];
  if (MODE != 2) {
#pragma unroll
    for (int i = 0; i < 5; ++i) {
      int r = row0 + (l + 4 * i) * 8 + (lane >> 3);
      r = r > SEQ - 1 ? SEQ - 1 : r;
      xo[i] = r * ROWB + (((lane & 7) ^ (lane >> 3)) << 4);
    }
  }
#pragma unroll
  for (int i = 0; i < 4; ++i) {
    const int kp = l + 4 * (i >> 1);
    const int k = kp * 8 + ((lane >> 2) & 7);
    wo[i] = k * ldw + ((i & 1) ? colB1 : colB0) + (lane >> 5) * 64 + (((lane & 3) ^ ((kp & 1) << 1)) << 4);
  }
  if (MODE == 1) {
#pragma unroll
    for (int i = 0; i < 4; ++i) {
      const int kp = l + 4 * (i >> 1);
      const int k = kp * 8 + (i & 1) * 4 + (lane >> 4);
      int dw = (lane & 15) ^ ((kp & 1) << 3);
      dw = dw > 8 ? 8 : dw;
      eo[i] = k * 36 + dw * 4;
    }
  }
  auto issue = [&](int j) {
    int ks = j + rot; ks = ks >= 9 ? ks - 9 : ks;
    const unsigned sb = lds0 + (j & 3) * K1_STAGE;
    if (MODE != 2) {
      const char* xb = X + ks * 128;
#pragma unroll
      for (int i = 0; i < 5; ++i) glds16(xo[i], xb, sb + (l + 4 * i) * 1024);
    }
    const char* w0 = W0 + (size_t)ks * 64 * ldw;
    const char* w1 = W1 + (size_t)ks * 64 * ldw;
#pragma unroll
    for (int i = 0; i < 4; ++i) {
      if (MODE == 1 && (i & 1)) continue;
      glds16(wo[i], (i & 1) ? w1 : w0, sb + K1_XB + (l + 4 * (i >> 1)) * 2048 + (i & 1) * 1024);
    }
    if (MODE == 1) {
      const char* kb = wk + ks * (64 * 36);
      const char* vb = wv + ks * (64 * 36);
#pragma unroll
      for (int i = 0; i < 4; ++i) {
        const unsigned d = sb + K1_XB + (l + 4 * (i >> 1)) * 2048 + 1024 + (i & 1) * 256;
        glds4(eo[i], kb, d);
        glds4(eo[i], vb, d + 512);
      }
    }
  };
  constexpr int P = MODE == 0 ? 9 : (MODE == 1 ? 15 : 4);
  issue(0); issue(1);
  wait_vm<P>(); bar_lgkm();
  issue(2);
#pragma unroll 1
  for (int t = 3; t < 9; ++t) { issue(t); wait_vm<2 * P>(); bar_lgkm(); }
  wait_vm<P>(); bar_lgkm();
  wait_vm<0>(); bar_lgkm();
  bar_lgkm();
}

__global__ __launch_bounds__(512) void k1_proj(const char* __restrict__ xA, const char* __restrict__ wqkv,
                                               const char* __restrict__ xB, const char* __restrict__ wq,
                                               const char* __restrict__ wk, const char* __restrict__ wv,
                                               const char* __restrict__ woA, const char* __restrict__ woB,
                                               char* __restrict__ ws) {
  extern __shared__ __attribute__((aligned(16))) char smem[];
  asm volatile("" ::"s"(xA), "s"(wqkv), "s"(woA), "s"(xB), "s"(wq), "s"(wk), "s"(wv), "s"(woB), "s"(ws));
  const int tid = threadIdx.x, lane = tid & 63;
  const int wave = __builtin_amdgcn_readfirstlane(tid >> 6);

  const unsigned lds0 = (unsigned)(uintptr_t)smem;
  const int b = blockIdx.x;
  const int unit = (b & 7) * 32 + (b >> 3);
  const int fr = lane & 15, kg = lane >> 4;
  const int rot = unit % 9;
  const int tro = kg * 2048 + (fr >> 2) * 64 + (fr & 3) * 8;
  const int trE = K1_XB + tro + ((kg & 1) << 5), trO = K1_XB + tro + (((kg & 1) ^ 1) << 5);

  if (__builtin_expect(unit >= K1_GEMM, 0)) {
    const int s = unit - K1_GEMM;
    if (s == 0 && tid < 256) *(unsigned*)(ws + WS_CNT + tid * 128) = 0u;
    if (wave >= 4) {
      const int c0 = s * 128, c1 = c0 + 64;
      k1_loader<2>(nullptr, 0, c0 < DM ? woA : woB, c1 < DM ? woA : woB, ROWB, (c0 < DM ? c0 : c0 - DM) * 2, (c1 < DM ? c1 : c1 - DM) * 2,
                   nullptr, nullptr, rot, lds0, wave - 4, lane);
    } else {
      bar_lgkm();
      char* dst = ws + WS_WF + (size_t)(s * 8 + wave * 2) * 18432 + lane * 16;
#pragma unroll 1
      for (int j = 0; j < 9; ++j) {
        int ks = j + rot; ks = ks >= 9 ? ks - 9 : ks;
        const char* sb = smem + (j & 3) * K1_STAGE + wave * 512;
        const bf16x8 f0 = tr_frag(sb + trE), f1 = tr_frag(sb + trO), f2 = tr_frag(sb + trE + 8192), f3 = tr_frag(sb + trO + 8192);
        bar_lgkm();
        ST16(dst + ks * 2048, __builtin_bit_cast(u32x4, f0));
        ST16(dst + ks * 2048 + 18432, __builtin_bit_cast(u32x4, f1));
        ST16(dst + ks * 2048 + 1024, __builtin_bit_cast(u32x4, f2));
        ST16(dst + ks * 2048 + 18432 + 1024, __builtin_bit_cast(u32x4, f3));
      }
    }

    return;
  }
  const bool isA = unit < K1_RB * K1_ACT;
  const int u5 = isA ? unit : unit - K1_RB * K1_ACT, d5 = (u5 * 205) >> 10, m5 = u5 - d5 * 5;
  const int v4 = unit - (unit >= 126 ? 126 : 70);
  const bool five = !isA || unit < 70;
  const int ct = five ? (isA ? d5 : m5) : v4 >> 2;
  const int rb = five ? (isA ? m5 : d5) : (unit >= 126 ? 9 : 5) + (v4 & 3);
  const int row0 = rb * 160;
  const bool last = ct == (isA ? K1_ACT - 1 : K1_BCT - 1);
  const int wm = wave & 1, wn = (wave >> 1) & 1;
  constexpr int ST_LD = 144;
  if (__builtin_expect(wave >= 4, 1)) {
    if (__builtin_expect(isA || !last, 1))
      k1_loader<0>(isA ? xA : xB, row0, isA ? wqkv : wq, isA ? wqkv : wq, isA ? 3 * ROWB : ROWB, ct * 256, ct * 256 + (last ? 0 : 128),
                   nullptr, nullptr, rot, lds0, wave - 4, lane);
    else k1_loader<1>(xB, row0, wq, wq, ROWB, ct * 256, 0, wk, wv, rot, lds0, wave - 4, lane);
  } else {
    f32x4 acc[5][4] = {};
    const int xo0 = (wm * 80 + fr) * 128 + ((kg ^ (fr & 7)) << 4), xo1 = xo0 ^ 64;
    const int wE = trE + wn * 1024, wO = trO + wn * 1024;
    bf16x8 xa[5], wa[4], xb[5], wb[4];
    auto rd = [&](const char* sb, int kk, bf16x8 (&xf)[5], bf16x8 (&wf)[4]) {
#pragma unroll
      for (int m = 0; m < 5; ++m) xf[m] = *(const bf16x8*)(sb + (kk ? xo1 : xo0) + m * 2048);
#pragma unroll
      for (int n = 0; n < 4; ++n) wf[n] = tr_frag(sb + ((n & 1) ? wO : wE) + kk * 8192 + (n >> 1) * 512);
    };
    auto mm = [&](bf16x8 (&xf)[5], bf16x8 (&wf)[4]) {
#pragma unroll
      for (int n = 0; n < 4; ++n)
#pragma unroll
        for (int m = 0; m < 5; ++m) acc[m][n] = MFMA16(wf[n], xf[m], acc[m][n]);
    };
    bar_lgkm();

    rd(smem, 0, xa, wa);
#define ILV() do { _Pragma("unroll") for (int q_ = 0; q_ < 13; ++q_) { __builtin_amdgcn_sched_group_barrier(0x8, 1, 0); __builtin_amdgcn_sched_group_barrier(0x100, 1, 0); } \
                   __builtin_amdgcn_sched_group_barrier(0x8, 7, 0); } while (0)
    K1UNROLL
    for (int j = 0; j < 9; ++j) {
      rd(smem + (j & 3) * K1_STAGE, 1, xb, wb);
      mm(xa, wa);
      ILV();
      __builtin_amdgcn_sched_barrier(0);
      bar_lgkm();
      rd(smem + ((j + 1) & 3) * K1_STAGE, 0, xa, wa);
      mm(xb, wb);
      ILV();
      __builtin_amdgcn_sched_barrier(0);
    }

    char* st = smem + wave * (80 * ST_LD);
#pragma unroll
    for (int m = 0; m < 5; ++m)
#pragma unroll
      for (int n = 0; n < 4; ++n) {
        u32x2 u = {pk2(acc[m][n][0], acc[m][n][1]), pk2(acc[m][n][2], acc[m][n][3])};
        *(u32x2*)(st + (m * 16 + fr) * ST_LD + n * 32 + kg * 8) = u;
      }
    asm volatile("s_waitcnt lgkmcnt(0)" ::: "memory");

    if (__builtin_expect(!(last && wn == 1), 1)) {
      const int ld = isA ? 3 * ROWB : ROWB, r0 = row0 + wm * 80 + (lane >> 3);
      char* d = (isA ? ws + WS_QKV : ws + WS_QB) + (size_t)r0 * ld + ct * 256 + wn * 128 + (lane & 7) * 16;
      u32x4 v[10];
#pragma unroll
      for (int i = 0; i < 10; ++i) v[i] = *(const u32x4*)(st + (i * 8 + (lane >> 3)) * ST_LD + (lane & 7) * 16);
#pragma unroll
      for (int i = 0; i < 10; ++i) if (r0 + i * 8 < SEQ) ST16K(d + (size_t)i * 8 * ld, v[i]);
    }
  }
  if (__builtin_expect(!isA && last, 0)) {
    bar_lgkm();
    const char* st = smem + 2 * (80 * ST_LD);
    char* kv = ws + WS_KV;
    if (tid < 320) {
      const int r = tid >> 1, hh = tid & 1, key = row0 + r;
      if (key < SEQ) {
        const int t = key >> 5, k32 = key & 31;
        char* g = kv + (t >> 2) * GRP_B;
        *(u32x4*)(g + (t & 3) * 1024 + (hh * 32 + k32) * 16) = *(const u32x4*)(st + r * ST_LD + hh * 16);
        if (hh == 0) *(unsigned*)(g + KR_OFF + k32 * 16 + (t & 3) * 4) = *(const unsigned*)(st + r * ST_LD + 32);
      }
    }
    const int t2 = 511 - tid;
    if (t2 < 400) {
      const int blk = t2 / 20, d = t2 - blk * 20;
      const int tl = blk >> 2, s = (blk >> 1) & 1, hh = blk & 1;
      const int t = (row0 >> 5) + tl;
      if (t < 64) {
        const char* src = st + (tl * 32 + s * 16 + hh * 4) * ST_LD + 64 + (d < 18 ? d : 0) * 2;
        u32x4 o;
#pragma unroll
        for (int e = 0; e < 4; ++e) {
          const int r = (e >> 1) * 8 + (e & 1) * 2;
          o[e] = (unsigned)*(const unsigned short*)(src + r * ST_LD) | ((unsigned)*(const unsigned short*)(src + (r + 1) * ST_LD) << 16);
          o[e] = d < 18 ? o[e] : (d == 18 ? 0x3f803f80u : 0u);
        }
        *(u32x4*)(kv + (t >> 2) * GRP_B + V_OFF + (t & 3) * VT_B + (s * 2 + hh) * 320 + d * 16) = o;
      }
    }
  }

}

constexpr int K2_LDS = KV_BYTES + 6144;
#define MOFF 96.0f
#define NSAMP 1
constexpr float M_OFF = MOFF;
#define SB __builtin_amdgcn_sched_barrier(0)
#define MF16(a, b, c) __builtin_amdgcn_mfma_f32_32x32x16_bf16(a, b, c, 0, 0, 0)
#define MF8(a, b, c) __builtin_amdgcn_mfma_f32_32x32x8bf16_1k(a, b, c, 0, 0, 0)
#define EX(i) cur[i] = __builtin_amdgcn_exp2f(cur[i])

DI float xhalf(float x);
DI float max16(const f32x16& s, float m) {
#pragma unroll
  for (int i = 0; i < 16; i += 2) m = __builtin_fmaxf(__builtin_fmaxf(s[i], s[i + 1]), m);
  return m;
}
#define E(i) EX(i); SB;
#define C0(i) p0[i] = pk2(cur[2 * i], cur[2 * i + 1]); SB;
#define C1(i) p1[i] = pk2(cur[8 + 2 * i], cur[9 + 2 * i]); SB;
template <bool RELOAD>
DI void phase(f32x16& cur, f32x16& nxt, s16x4& kra, const char* krp, const s16x4 qr, bf16x8& kf, const char* kp2, const bf16x8 qf,
              bf16x8& v0, const char* v0p, bf16x8& v1, const char* v1p, u32x4& p0, u32x4& p1, f32x16& o) {
  const f32x16 z = {};
  nxt = MF8(kra, qr, z); SB;
  if (RELOAD) { kra = *(const s16x4*)krp; SB; }
  E(0) E(1) E(2) E(3) C0(0) E(4)
  o = MF16(v1, __builtin_bit_cast(bf16x8, p1), o); SB;
  v1 = *(const bf16x8*)v1p; SB;
  C0(1) E(5) E(6) E(7) C0(2) E(8)
  nxt = MF16(kf, qf, nxt); SB;
  kf = *(const bf16x8*)kp2; SB;
  C0(3) E(9) E(10) E(11) C1(0) E(12)
  o = MF16(v0, __builtin_bit_cast(bf16x8, p0), o); SB;
  v0 = *(const bf16x8*)v0p; SB;
  C1(1) E(13) E(14) E(15) C1(2) C1(3)
}
DI unsigned rem_word(unsigned qrem, float m, int h) {
  const unsigned hi = pk2(-m, 0.f) & 0xffffu;
  const unsigned lo = pk2(-m - bf_lo(hi), 0.f) & 0xffffu;
  return h ? (hi | (lo << 16)) : qrem;
}
DI s16x4 mk2(unsigned a, unsigned b) { u32x2 r = {a, b}; return __builtin_bit_cast(s16x4, r); }

DI float max_pass(const char* kp, const char* krp, const bf16x8 qf, unsigned rw, int npair) {
  const s16x4 qe = mk2(rw, 0u), qo = mk2(0u, rw);
  const f32x16 z = {};
  float m = -3.0e38f;
  for (int i = 0; i < npair; ++i) {
    const int G = (i >> 1) * GRP_B, hf = i & 1;
    const s16x4 kr = *(const s16x4*)(krp + G + hf * 8);
    m = max16(MF16(*(const bf16x8*)(kp + G + hf * 2048), qf, MF8(kr, qe, z)), m);
    m = max16(MF16(*(const bf16x8*)(kp + G + hf * 2048 + 1024), qf, MF8(kr, qo, z)), m);
  }
  return fmaxf(m, xhalf(m));
}
DI void attn_exact(const char* img, int ko, int kro, int vo, const bf16x8 qf, unsigned qrem, int h, f32x16& o) {
  const float mt = max_pass(img + ko, img + kro, qf, rem_word(qrem, 0.f, h), 32);
  const unsigned rw = rem_word(qrem, mt, h);
  const s16x4 qe = mk2(rw, 0u), qo = mk2(0u, rw);
  const f32x16 z = {};
  o = z;
  for (int t = 0; t < 64; ++t) {
    const char* g = img + (t >> 2) * GRP_B;
    const s16x4 kr = *(const s16x4*)(g + kro + (t & 2) * 4);
    f32x16 cur = MF16(*(const bf16x8*)(g + ko + (t & 3) * 1024), qf, MF8(kr, (t & 1) ? qo : qe, z));
    u32x4 p0, p1;
#pragma unroll
    for (int i = 0; i < 16; ++i) EX(i);
#pragma unroll
    for (int i = 0; i < 4; ++i) { p0[i] = pk2(cur[2 * i], cur[2 * i + 1]); p1[i] = pk2(cur[8 + 2 * i], cur[9 + 2 * i]); }
    o = MF16(*(const bf16x8*)(g + vo + (t & 3) * VT_B), __builtin_bit_cast(bf16x8, p0), o);
    o = MF16(*(const bf16x8*)(g + vo + (t & 3) * VT_B + 640), __builtin_bit_cast(bf16x8, p1), o);
  }
}

constexpr int LA_0 = 98304;
constexpr int LA_Q = LA_0 + 22 * 2304;
constexpr int L_OB = 94208;
constexpr int L_P = 104448;
constexpr int P_LD = 272, P_HALF = 80 * P_LD;
constexpr int XC_B = 10240;
constexpr int L_CTL = K2_LDS;
constexpr int K2_LDS_ALL = K2_LDS + 64;
enum { C_KVA = 0, C_KVB = 4, C_PAST = 8, C_AROWS = 12, C_A2 = 16, C_XA = 20, C_DONE = 24, C_PROG = 32 };

DI void lds_add(unsigned addr) {
  asm volatile("s_waitcnt lgkmcnt(0)\n\ts_mov_b64 exec, 1\n\tds_add_u32 %0, %1\n\ts_mov_b64 exec, -1" ::"v"(addr), "v"(1u) : "memory");
}
template <int N> DI void lds_wait(unsigned addr) {
  unsigned r, t;
  asm volatile("1:\n\tds_read_b32 %0, %2\n\ts_waitcnt lgkmcnt(0)\n\tv_readfirstlane_b32 %1, %0\n\ts_cmp_lt_u32 %1, %3\n\ts_cbranch_scc1 1b"
               : "=&v"(r), "=&s"(t) : "v"(addr), "i"(N) : "memory", "scc");
}
DI void cnt_wait(const char* p) {
  unsigned r, n;
  asm volatile("s_mov_b32 %1, 0x400000\n1:\n\tglobal_load_dword %0, %2, off sc1\n\ts_sub_u32 %1, %1, 1\n\ts_cbranch_scc1 2f\n\t"
               "s_waitcnt vmcnt(0)\n\tv_cmp_gt_u32 vcc, 16, %0\n\ts_cbranch_vccz 2f\n\ts_sleep " POLL_SLEEP "\n\ts_branch 1b\n2:\n\ts_waitcnt vmcnt(0)"
               : "=&v"(r), "=&s"(n) : "v"(p) : "memory", "scc", "vcc");
}
DI float xhalf(float x) {
  const unsigned u = __float_as_uint(x);
  const auto r = __builtin_amdgcn_permlane32_swap(u, u, false, false);
  return __uint_as_float((threadIdx.x & 32) ? r[0] : r[1]);
}
DI void local_attn(char* smem, unsigned lds0, int wave, int c32, int h, int row) {
  unsigned* qp = (unsigned*)(smem + LA_Q + wave * ROWB + c32 * 36);
  unsigned q[9];
#pragma unroll
  for (int e = 0; e < 9; ++e) q[e] = qp[e];
  const unsigned kb = lds0 + LA_0 + (wave + 8 - h * 8) * 2304 + c32 * 36;
  const unsigned vb = lds0 + LA_0 + wave * 2304 + ROWB + c32 * 36 + h * 16;
  u32x2 kk[9][2], vv[5][4];
#pragma unroll
  for (int e = 0; e < 9; ++e)
    asm volatile("ds_read2st64_b32 %0, %2 offset0:54 offset1:36\n\tds_read2st64_b32 %1, %2 offset0:18 offset1:0"
                 : "=&v"(kk[e][0]), "=&v"(kk[e][1]) : "v"(kb + e * 4) : "memory");
  asm volatile("s_waitcnt lgkmcnt(0)" : "+v"(kk[0][0]), "+v"(kk[0][1]), "+v"(kk[1][0]), "+v"(kk[1][1]), "+v"(kk[2][0]), "+v"(kk[2][1]),
      "+v"(kk[3][0]), "+v"(kk[3][1]), "+v"(kk[4][0]), "+v"(kk[4][1]), "+v"(kk[5][0]), "+v"(kk[5][1]), "+v"(kk[6][0]), "+v"(kk[6][1]),
      "+v"(kk[7][0]), "+v"(kk[7][1]), "+v"(kk[8][0]), "+v"(kk[8][1])::"memory");
#pragma unroll
  for (int e = 0; e < 5; ++e)
    asm volatile("ds_read2st64_b32 %0, %4 offset0:126 offset1:108\n\tds_read2st64_b32 %1, %4 offset0:90 offset1:72\n\t"
                 "ds_read2st64_b32 %2, %4 offset0:54 offset1:36\n\tds_read2st64_b32 %3, %4 offset0:18 offset1:0"
                 : "=&v"(vv[e][0]), "=&v"(vv[e][1]), "=&v"(vv[e][2]), "=&v"(vv[e][3]) : "v"(vb + e * 4) : "memory");
  float lg[4] = {0.f, 0.f, 0.f, 0.f};
#pragma unroll
  for (int e = 0; e < 9; ++e) {
    const unsigned k0 = kk[e][0][0], k1 = kk[e][0][1], k2 = kk[e][1][0], k3 = kk[e][1][1];
    lg[0] = dot2(q[e], k0, lg[0]); lg[1] = dot2(q[e], k1, lg[1]); lg[2] = dot2(q[e], k2, lg[2]); lg[3] = dot2(q[e], k3, lg[3]);
  }
#pragma unroll
  for (int w = 0; w < 4; ++w) lg[w] = row - 2 * (h * 4 + w) >= 0 ? lg[w] * QSCALE : -1.0e9f * LOG2E;
  float mx = fmaxf(fmaxf(lg[0], lg[1]), fmaxf(lg[2], lg[3]));
  mx = fmaxf(mx, xhalf(mx));
  float sum = 0.f;
#pragma unroll
  for (int w = 0; w < 4; ++w) { lg[w] = __builtin_amdgcn_exp2f(lg[w] - mx); sum += lg[w]; }
  sum += xhalf(sum);
  const float inv = __builtin_amdgcn_rcpf(sum);
  const unsigned pa = pk2(lg[0] * inv, lg[1] * inv), pb = pk2(lg[2] * inv, lg[3] * inv);
  const auto ra = __builtin_amdgcn_permlane32_swap(pa, pa, false, false);
  const auto rb = __builtin_amdgcn_permlane32_swap(pb, pb, false, false);
  const unsigned pp[4] = {ra[0], rb[0], ra[1], rb[1]};
  asm volatile("s_waitcnt lgkmcnt(0)" : "+v"(vv[0][0]), "+v"(vv[0][1]), "+v"(vv[0][2]), "+v"(vv[0][3]), "+v"(vv[1][0]), "+v"(vv[1][1]),
      "+v"(vv[1][2]), "+v"(vv[1][3]), "+v"(vv[2][0]), "+v"(vv[2][1]), "+v"(vv[2][2]), "+v"(vv[2][3]), "+v"(vv[3][0]), "+v"(vv[3][1]),
      "+v"(vv[3][2]), "+v"(vv[3][3]), "+v"(vv[4][0]), "+v"(vv[4][1]), "+v"(vv[4][2]), "+v"(vv[4][3])::"memory");
#pragma unroll
  for (int e = 0; e < 5; ++e) {
    float o0 = 0.f, o1 = 0.f;
#pragma unroll
    for (int w2 = 0; w2 < 4; ++w2) {
      const unsigned x = vv[e][w2][0], y = vv[e][w2][1];
      o0 = dot2(__builtin_amdgcn_perm(y, x, 0x05040100u), pp[w2], o0);
      o1 = dot2(__builtin_amdgcn_perm(y, x, 0x07060302u), pp[w2], o1);
    }
    qp[h * 4 + e] = pk2(o0, o1);
  }
}
template <int MASK> DI void glds16_part(unsigned voff, const void* sbase, unsigned lds_dst) {
  unsigned keep;
  asm volatile("s_mov_b32 %0, m0\n\ts_mov_b32 m0, %3\n\ts_mov_b64 exec, %4\n\tglobal_load_lds_dwordx4 %1, %2\n\ts_mov_b64 exec, -1\n\ts_mov_b32 m0, %0"
               : "=&s"(keep) : "v"(voff), "s"(sbase), "s"(lds_dst), "i"(MASK) : "memory");
}
DI void glds16_sc1(unsigned voff, const void* sbase, unsigned lds_dst) {
  unsigned keep;
  asm volatile("s_mov_b32 %0, m0\n\ts_mov_b32 m0, %3\n\ts_nop 0\n\tglobal_load_lds_dwordx4 %1, %2 sc1\n\ts_mov_b32 m0, %0"
               : "=&s"(keep) : "v"(voff), "s"(sbase), "s"(lds_dst) : "memory");
}
DI void store_row(const char* stage, char* dst, int lane) {
  asm volatile("s_waitcnt lgkmcnt(0)" ::: "memory");
  const u32x4 a = *(const u32x4*)(stage + lane * 16);
  const u32x4 b = *(const u32x4*)(stage + 1024 + (lane & 7) * 16);
  asm volatile("global_store_dwordx4 %0, %1, off sc1\n\ts_mov_b64 exec, 0xff\n\tglobal_store_dwordx4 %0, %2, off offset:1024 sc1\n\t"
               "s_mov_b64 exec, -1\n\ts_nop 0" ::"v"(dst + lane * 16), "v"(a), "v"(b) : "memory");
}
DI void signal_row(char* cnt) {
  asm volatile("s_waitcnt vmcnt(0)\n\ts_mov_b64 exec, 1\n\tglobal_atomic_add %0, %1, off sc1\n\ts_mov_b64 exec, -1" ::"v"(cnt), "v"(1u) : "memory");
}

__global__ __launch_bounds__(512) void k2_attn(char* __restrict__ ws, char* __restrict__ out) {
  extern __shared__ __attribute__((aligned(16))) char smem[];
  asm volatile("" ::"s"(ws), "s"(out));
  const int tid = threadIdx.x, lane = tid & 63;
  const int wave = __builtin_amdgcn_readfirstlane(tid >> 6);

  const int b = blockIdx.x;
  const int rblk = (b & 7) * 32 + (b >> 3);
  const int row = rblk * 8 + wave;
  const int c32 = lane & 31, h = lane >> 5;
  const unsigned lds0 = (unsigned)(uintptr_t)smem;
  const unsigned ctl = lds0 + L_CTL;
  const int ko = lane * 16;
  auto kv_fetch = [&](int p0, int step, int n) {
#pragma unroll
    for (int i = 0; i < n; ++i) glds16(ko, ws + WS_KV + (p0 + step * i) * 1024, lds0 + (p0 + step * i) * 1024);
  };
  u32x4 qv;
  unsigned qrem;
  {
    const char* qp = ws + WS_QB + (size_t)row * ROWB + c32 * 36;
    asm volatile("global_load_dwordx4 %0, %1, off" : "=v"(qv) : "v"(qp + h * 16) : "memory");
    asm volatile("global_load_dword %0, %1, off offset:32" : "=v"(qrem) : "v"(qp) : "memory");
    kv_fetch(wave, 8, 5);
  }
  if (tid < 16) *(unsigned*)(smem + L_CTL + tid * 4) = 0u;
  const int vrow = c32 < 19 ? c32 : (c32 == 28 ? 18 : 19);
  const int kro = h ? V_OFF + 18 * 16 : KR_OFF + c32 * 16;
  const int vo = V_OFF + h * 320 + vrow * 16;
  const int rg = rblk / 9, cg = rblk - rg * 9;
  const int nrt = rg < 16 ? 5 : (rg < 28 ? 4 : 0), rt0 = rg < 16 ? rg * 5 : (rg < 28 ? 16 + rg * 4 : 0);
  const int fr = lane & 15, kg = lane >> 4;
  const unsigned xo = (lane >> 3) * ROWB + (((lane & 7) ^ (lane >> 3)) << 4);
  auto fetch_tile = [&](const char* src) {
    const char* s0 = src + (size_t)(rt0 * 16 + wave * 8) * ROWB;
    const char* s1 = src + (size_t)(rt0 * 16 + (nrt == 5 ? 64 : 0) + (wave & 1) * 8) * ROWB;
#pragma unroll
    for (int c = 0; c < 9; ++c) {
      glds16_sc1(xo, s0 + c * 128, lds0 + c * XC_B + wave * 1024);
      if (c % 3 == 2) {
        const int ce = c == 8 ? 8 : (c / 3) * 4 + (wave >> 1);
        glds16_sc1(xo, s1 + ce * 128, lds0 + ce * XC_B + (8 + (wave & 1)) * 1024);
      }
    }
  };
  const char* cnt_p = ws + WS_CNT + (rt0 + (lane < nrt ? lane : 0)) * 128;
  const char* wfa = ws + WS_WF + (size_t)(cg * 4 + (wave & 3)) * 18432 + (wave >> 2) * 1024;
  const f32x16 z = {};
  f32x16 o = z;
  u32x4 wA[9], wB[9];

  asm volatile("s_waitcnt vmcnt(5)" : "+v"(qv), "+v"(qrem)::"memory");
#pragma unroll
  for (int e = 0; e < 4; ++e) { const unsigned u = qv[e]; qv[e] = pk2(bf_lo(u) * QSCALE, bf_hi(u) * QSCALE); }
  qrem = pk2(bf_lo(qrem) * QSCALE, bf_hi(qrem) * QSCALE);
  const bf16x8 qf = __builtin_bit_cast(bf16x8, qv);
  wait_vm<0>();
  bar_lgkm();

  if (wave >= 4) {
    const int w4 = wave - 4;
    kv_fetch(40 + w4, 4, 14);
#pragma unroll
    for (int i = 0; i < 6; ++i) {
      int r = w4 + 4 * i;
      r = r > 21 ? 21 : r;
      const int gr = rblk * 8 + r - 14;
      const char* src = ws + WS_QKV + (size_t)(gr < 0 ? 0 : gr) * (3 * ROWB) + ROWB;
      glds16(ko, src, lds0 + LA_0 + r * 2304);
      glds16(ko, src + 1024, lds0 + LA_0 + r * 2304 + 1024);
      glds16_part<0xffff>(ko, src + 2048, lds0 + LA_0 + r * 2304 + 2048);
    }
#pragma unroll
    for (int i = 0; i < 2; ++i) {
      const char* src = ws + WS_QKV + (size_t)(rblk * 8 + w4 + 4 * i) * (3 * ROWB);
      glds16(ko, src, lds0 + LA_Q + (w4 + 4 * i) * ROWB);
      glds16_part<0xff>(ko, src + 1024, lds0 + LA_Q + (w4 + 4 * i) * ROWB + 1024);
    }
    wait_vm<22>();
    lds_add(ctl + C_KVA);
  }
  const float ms = max_pass(smem + ko, smem + kro, qf, rem_word(qrem, 0.f, h), NSAMP);
  const unsigned rw = rem_word(qrem, ms + M_OFF, h);
  {
    int kq = ko, krq = kro, vq = vo;
    const char *kp = smem + kq, *krp = smem + krq, *vp = smem + vq;
    const s16x4 qe = mk2(rw, 0u), qo = mk2(0u, rw);
    s16x4 k01 = *(const s16x4*)krp, k23 = *(const s16x4*)(krp + 8);
    f32x16 sa = MF16(*(const bf16x8*)kp, qf, MF8(k01, qe, z)), sb;
    bf16x8 kf = *(const bf16x8*)(kp + 1024), v0 = *(const bf16x8*)vp, v1 = v0;
    u32x4 p0 = {}, p1 = {};
    unsigned cnt_seen;

#define WF9(W, BASE) asm volatile( \
        "global_load_dwordx4 %0, %9, %10\n\tglobal_load_dwordx4 %1, %9, %10 offset:2048\n\t" \
        "global_load_dwordx4 %2, %11, %10 offset:-4096\n\tglobal_load_dwordx4 %3, %11, %10 offset:-2048\n\t" \
        "global_load_dwordx4 %4, %11, %10\n\tglobal_load_dwordx4 %5, %11, %10 offset:2048\n\t" \
        "global_load_dwordx4 %6, %12, %10 offset:-4096\n\tglobal_load_dwordx4 %7, %12, %10 offset:-2048\n\t" \
        "global_load_dwordx4 %8, %12, %10" \
        : "=&v"(W[0]), "=&v"(W[1]), "=&v"(W[2]), "=&v"(W[3]), "=&v"(W[4]), "=&v"(W[5]), "=&v"(W[6]), "=&v"(W[7]), "=&v"(W[8]) \
        : "v"(ko), "s"(BASE), "v"(ko + 8192), "v"(ko + 16384) : "memory")
    auto group = [&](auto gc) __attribute__((always_inline)) {
      constexpr int g = decltype(gc)::value;

      if constexpr (PRIO(g) != PRIO(g - 1)) __builtin_amdgcn_s_setprio(PRIO(g));
      if constexpr (g == 1) { wait_vm<0>(); lds_add(ctl + C_AROWS); }
      if constexpr (g == 3) lds_wait<4>(ctl + C_KVA);
      if constexpr (g == 3 || g == 4) {
        if ((wave >> 2) == 4 - g) {
          lds_wait<8>(ctl + C_AROWS);
          local_attn(smem, lds0, wave, c32, h, row);
          store_row(smem + LA_Q + wave * ROWB, ws + WS_OA + (size_t)row * ROWB, lane);
          lds_add(ctl + C_A2);
        }
      }
      if constexpr (g == 6) {
        signal_row(ws + WS_CNT + (row >> 4) * 128);
        lds_wait<8>(ctl + C_A2);
        kv_fetch(96 + wave, 8, 7);
      }
      if constexpr (g == 7) { wait_vm<0>(); lds_add(ctl + C_KVB); }
      if constexpr (g == 8) WF9(wA, wfa);
      if constexpr (g == 9) lds_wait<8>(ctl + C_KVB);
      if constexpr (g == 10) WF9(wB, wfa + 36 * 18432);
      if constexpr (g == 11) lds_add(ctl + C_PAST);
      if constexpr (g == 12) asm volatile("global_load_dword %0, %1, off sc1" : "=v"(cnt_seen) : "v"(cnt_p) : "memory");
      if constexpr (g == 13) {
        lds_wait<8>(ctl + C_PAST);
        asm volatile("s_waitcnt vmcnt(0)" : "+v"(cnt_seen)::"memory");
        if (__builtin_expect(!__all(cnt_seen >= 16u), 0)) cnt_wait(cnt_p);
        fetch_tile(ws + WS_OA);
      }
      if constexpr (g == 15) { wait_vm<0>(); lds_add(ctl + C_XA); }

      SB;
      const int G = (g & 3) * GRP_B;
      phase<true>(sa, sb, k01, krp + G + GRP_B, qo, kf, kp + G + 2048, qf, v0, vp + G + VT_B, v1, vp + G + 640, p0, p1, o);
      phase<false>(sb, sa, k23, nullptr, qe, kf, kp + G + 3072, qf, v0, vp + G + 2 * VT_B, v1, vp + G + VT_B + 640, p0, p1, o);
      phase<true>(sa, sb, k23, krp + G + GRP_B + 8, qo, kf, kp + G + GRP_B, qf, v0, vp + G + 3 * VT_B, v1, vp + G + 2 * VT_B + 640, p0, p1, o);
      phase<false>(sb, sa, k01, nullptr, qe, kf, kp + G + GRP_B + 1024, qf, v0, vp + G + GRP_B, v1, vp + G + 3 * VT_B + 640, p0, p1, o);
      asm volatile("" : "+v"(o), "+v"(sa), "+v"(p1));
      if constexpr ((g & 3) == 3) {
        kq += 4 * GRP_B; krq += 4 * GRP_B; vq += 4 * GRP_B;
        asm volatile("" : "+v"(kq), "+v"(krq), "+v"(vq));
        kp = smem + kq; krp = smem + krq; vp = smem + vq;
      }
    };
#define GR(i) group(IC<i>{});
    GR(0) GR(1) GR(2) GR(3) GR(4) GR(5) GR(6) GR(7) GR(8) GR(9) GR(10) GR(11) GR(12) GR(13) GR(14) GR(15)
    o = MF16(v1, __builtin_bit_cast(bf16x8, p1), o);
  }

  lds_add(ctl + C_DONE);
  float l = h ? o[12] : o[10];
  float big = l;
#pragma unroll
  for (int i = 0; i < 10; ++i) big += fabsf(o[i]);
  const bool ok = big < 1.0e37f && l > 1.0e-33f;
  if (__builtin_expect(!__all(ok), 0)) {
    attn_exact(ws + WS_KV, ko, kro, vo, qf, qrem, h, o);
    l = h ? o[12] : o[10];
  }
  {
    const float inv = __builtin_amdgcn_rcpf(l);
    unsigned* dst = (unsigned*)(smem + L_OB + wave * ROWB + c32 * 36 + h * 8);
    dst[0] = pk2(o[0] * inv, o[1] * inv);
    dst[1] = pk2(o[2] * inv, o[3] * inv);
    dst[4] = pk2(o[4] * inv, o[5] * inv);
    dst[5] = pk2(o[6] * inv, o[7] * inv);
    if (!h) dst[8] = pk2(o[8] * inv, o[9] * inv);
    store_row(smem + L_OB + wave * ROWB, ws + WS_OB + (size_t)row * ROWB, lane);
  }
  char* cntB = ws + WS_CNT + (128 + (row >> 4)) * 128;
  const int xa = fr * 128 + ((((wave >> 2) * 4 + kg) ^ (fr & 7)) << 4);
  char* pw = smem + L_P + (wave >> 2) * P_HALF + fr * P_LD + (wave & 3) * 64 + kg * 16;
  f32x4 acc[5];
  auto mm3 = [&](u32x4 (&w)[9], int c0) {
    bf16x8 xf[3][5];
#pragma unroll
    for (int c = 0; c < 3; ++c)
#pragma unroll
      for (int m = 0; m < 5; ++m) xf[c][m] = *(const bf16x8*)(smem + (c0 + c) * XC_B + xa + m * 2048);
    SB;
#pragma unroll
    for (int c = 0; c < 3; ++c)
#pragma unroll
      for (int m = 0; m < 5; ++m) acc[m] = MFMA16(__builtin_bit_cast(bf16x8, w[c0 + c]), xf[c][m], (c0 + c) ? acc[m] : f32x4{});
  };
  auto put = [&] {
#pragma unroll
    for (int m = 0; m < 5; ++m) *(f32x4*)(pw + m * 16 * P_LD) = acc[m];
  };
  auto tile_out = [&](int br) {
#pragma unroll
    for (int i = 0; i < 2; ++i) {
      const int r = (tid >> 3) + 64 * i, c = tid & 7;
      if (r < nrt * 16) {
        const char* p = smem + L_P + r * P_LD + c * 32;
        const f32x4 a0 = *(const f32x4*)p, a1 = *(const f32x4*)(p + 16);
        const f32x4 b0 = *(const f32x4*)(p + P_HALF), b1 = *(const f32x4*)(p + P_HALF + 16);
        const u32x4 u = {pk2(a0[0] + b0[0], a0[1] + b0[1]), pk2(a0[2] + b0[2], a0[3] + b0[3]),
                         pk2(a1[0] + b1[0], a1[1] + b1[1]), pk2(a1[2] + b1[2], a1[3] + b1[3])};
        OUTST(out + (size_t)(rt0 * 16 + r) * (2 * ROWB) + br * ROWB + cg * 128 + c * 16, u);
      }
    }
  };
  signal_row(cntB);

  lds_wait<8>(ctl + C_XA);
  mm3(wA, 0);
  mm3(wA, 3);
  mm3(wA, 6);

  lds_wait<8>(ctl + C_DONE);
  put();

  if (wave == 0) cnt_wait(cnt_p + 128 * 128);
  bar_lgkm();

  fetch_tile(ws + WS_OB);

  tile_out(0);

  wait_vm<8>();
  bar_lgkm();

  mm3(wB, 0);
  wait_vm<4>();
  bar_lgkm();
  mm3(wB, 3);
  wait_vm<0>();
  bar_lgkm();
  mm3(wB, 6);
  put();
  bar_lgkm();

  tile_out(1);

}

extern "C" void kernel_launch(void* const* d_in, const int* in_sizes, int n_in, void* d_out, int out_size,
                              void* d_ws, size_t ws_size, hipStream_t stream) {
  (void)in_sizes; (void)n_in; (void)out_size; (void)ws_size;
  (void)hipFuncSetAttribute((const void*)k1_proj, hipFuncAttributeMaxDynamicSharedMemorySize, K1_LDS);
  (void)hipFuncSetAttribute((const void*)k2_attn, hipFuncAttributeMaxDynamicSharedMemorySize, K2_LDS_ALL);
  char* ws = (char*)d_ws;
  k1_proj<<<256, 512, K1_LDS, stream>>>((const char*)d_in[0], (const char*)d_in[1], (const char*)d_in[3], (const char*)d_in[4],
                                        (const char*)d_in[5], (const char*)d_in[6], (const char*)d_in[2], (const char*)d_in[7], ws);
  k2_attn<<<256, 512, K2_LDS_ALL, stream>>>(ws, (char*)d_out);
}
```

```cpp
#include <hip/hip_runtime.h>
#include <hip/hip_bf16.h>
#include <stdint.h>

typedef __bf16 bf16x2 __attribute__((ext_vector_type(2)));
typedef __bf16 bf16x8 __attribute__((ext_vector_type(8)));
typedef short s16x4 __attribute__((ext_vector_type(4)));
typedef float f32x2 __attribute__((ext_vector_type(2)));
typedef float f32x4 __attribute__((ext_vector_type(4)));
typedef float f32x16 __attribute__((ext_vector_type(16)));
typedef unsigned u32x2 __attribute__((ext_vector_type(2)));
typedef unsigned u32x4 __attribute__((ext_vector_type(4)));
#define LDSP(T) __attribute__((address_space(3))) T*
#define DI __device__ __forceinline__

#define PRIO(g) ((g) < 0 ? -1 : (g) < 8 ? 3 - ((g) >> 1) : (g) < 11 ? 3 : (g) < 13 ? 2 : (g) < 15 ? 1 : 0)
#define POLL_SLEEP "4"
constexpr int SEQ = 2048, DM = 576;
constexpr int ROWB = DM * 2;
constexpr float LOG2E = 1.4426950408889634f;
constexpr float QSCALE = 0.23570226039551584f * LOG2E;

constexpr int GRP_B = 9728, KR_OFF = 4096, V_OFF = 4608, VT_B = 1280;
constexpr int KV_BYTES = 16 * GRP_B;
constexpr size_t WS_KV = 0;
constexpr size_t WS_QB = WS_KV + KV_BYTES;
constexpr size_t WS_QKV = WS_QB + (size_t)SEQ * ROWB;
constexpr size_t WS_OA = WS_QKV + (size_t)SEQ * 3 * ROWB;
constexpr size_t WS_OB = WS_OA + (size_t)SEQ * ROWB;
constexpr size_t WS_WF = WS_OB + (size_t)SEQ * ROWB;
constexpr size_t WS_CNT = WS_WF + (size_t)72 * 18432;

template <int N> struct IC { static constexpr int value = N; };
DI unsigned pk2(float a, float b) {
  f32x2 f = {a, b};
  return __builtin_bit_cast(unsigned, __builtin_convertvector(f, bf16x2));
}
DI float dot2(unsigned a, unsigned b, float c) {
  return __builtin_amdgcn_fdot2_f32_bf16(__builtin_bit_cast(bf16x2, a), __builtin_bit_cast(bf16x2, b), c, false);
}
DI float bf_lo(unsigned u) { return __uint_as_float(u << 16); }
DI float bf_hi(unsigned u) { return __uint_as_float(u & 0xffff0000u); }

DI void glds16(unsigned voff, const void* sbase, unsigned lds_dst) {
  unsigned keep;
  asm volatile("s_mov_b32 %0, m0\n\ts_mov_b32 m0, %3\n\ts_nop 0\n\tglobal_load_lds_dwordx4 %1, %2\n\ts_mov_b32 m0, %0"
               : "=&s"(keep) : "v"(voff), "s"(sbase), "s"(lds_dst) : "memory");
}
DI void glds4(unsigned voff, const void* sbase, unsigned lds_dst) {
  unsigned keep;
  asm volatile("s_mov_b32 %0, m0\n\ts_mov_b32 m0, %3\n\ts_nop 0\n\tglobal_load_lds_dword %1, %2\n\ts_mov_b32 m0, %0"
               : "=&s"(keep) : "v"(voff), "s"(sbase), "s"(lds_dst) : "memory");
}
template <int N> DI void wait_vm() { asm volatile("s_waitcnt vmcnt(%0)" ::"i"(N) : "memory"); }
DI void bar_lgkm() { asm volatile("s_waitcnt lgkmcnt(0)\n\ts_barrier" ::: "memory"); }
DI bf16x8 tr_frag(const char* p) {
  s16x4 lo = __builtin_amdgcn_ds_read_tr16_b64_v4i16((LDSP(s16x4))(p));
  s16x4 hi = __builtin_amdgcn_ds_read_tr16_b64_v4i16((LDSP(s16x4))(p + 256));
  return __builtin_bit_cast(bf16x8, __builtin_shufflevector(lo, hi, 0, 1, 2, 3, 4, 5, 6, 7));
}
DI void ST16K(char* p, u32x4 v) { asm volatile("global_store_dwordx4 %0, %1, off sc1\n\ts_nop 1" :: "v"(p), "v"(v) : "memory"); }
#define MFMA16(a, b, c) __builtin_amdgcn_mfma_f32_16x16x32_bf16(a, b, c, 0, 0, 0)

constexpr int K1_XB = 20480, K1_STAGE = 36864, K1_LDS = 4 * K1_STAGE;
constexpr int K1_RB = 13, K1_ACT = 14, K1_BCT = 5, K1_GEMM = K1_RB * (K1_ACT + K1_BCT);

template <int MODE>
DI void k1_loader(const char* X, int row0, const char* W0, const char* W1, int ldw, int colB0, int colB1,
                  const char* wk, const char* wv, int rot, unsigned lds0, int l, int lane) {
  unsigned xo[5], wo[4], eo[8];
  if (MODE != 2) {
#pragma unroll
    for (int i = 0; i < 5; ++i) {
      int r = row0 + (l + 4 * i) * 8 + (lane >> 3);
      r = r > SEQ - 1 ? SEQ - 1 : r;
      xo[i] = r * ROWB + (((lane & 7) ^ (lane >> 3)) << 4);
    }
  }
#pragma unroll
  for (int i = 0; i < 4; ++i) {
    const int kp = l + 4 * (i >> 1);
    const int k = kp * 8 + ((lane >> 2) & 7);
    wo[i] = k * ldw + ((i & 1) ? colB1 : colB0) + (lane >> 5) * 64 + (((lane & 3) ^ ((kp & 1) << 1)) << 4);
  }
  if (MODE == 1) {
#pragma unroll
    for (int i = 0; i < 4; ++i) {
      const int kp = l + 4 * (i >> 1);
      const int k = kp * 8 + (i & 1) * 4 + (lane >> 4);
      int dw = (lane & 15) ^ ((kp & 1) << 3);
      dw = dw > 8 ? 8 : dw;
      eo[i] = k * 36 + dw * 4;
    }
  }
  auto issue = [&](int j) {
    int ks = j + rot; ks = ks >= 9 ? ks - 9 : ks;
    const unsigned sb = lds0 + (j & 3) * K1_STAGE;
    if (MODE != 2) {
      const char* xb = X + ks * 128;
#pragma unroll
      for (int i = 0; i < 5; ++i) glds16(xo[i], xb, sb + (l + 4 * i) * 1024);
    }
    const char* w0 = W0 + (size_t)ks * 64 * ldw;
    const char* w1 = W1 + (size_t)ks * 64 * ldw;
#pragma unroll
    for (int i = 0; i < 4; ++i) {
      if (MODE == 1 && (i & 1)) continue;
      glds16(wo[i], (i & 1) ? w1 : w0, sb + K1_XB + (l + 4 * (i >> 1)) * 2048 + (i & 1) * 1024);
    }
    if (MODE == 1) {
      const char* kb = wk + ks * (64 * 36);
      const char* vb = wv + ks * (64 * 36);
#pragma unroll
      for (int i = 0; i < 4; ++i) {
        const unsigned d = sb + K1_XB + (l + 4 * (i >> 1)) * 2048 + 1024 + (i & 1) * 256;
        glds4(eo[i], kb, d);
        glds4(eo[i], vb, d + 512);
      }
    }
  };
  constexpr int P = MODE == 0 ? 9 : (MODE == 1 ? 15 : 4);
  issue(0); issue(1);
  wait_vm<P>(); bar_lgkm();
  issue(2);
#pragma unroll 1
  for (int t = 3; t < 9; ++t) { issue(t); wait_vm<2 * P>(); bar_lgkm(); }
  wait_vm<P>(); bar_lgkm();
  wait_vm<0>(); bar_lgkm();
  bar_lgkm();
}

__global__ __launch_bounds__(512) void k1_proj(const char* __restrict__ xA, const char* __restrict__ wqkv,
                                               const char* __restrict__ xB, const char* __restrict__ wq,
                                               const char* __restrict__ wk, const char* __restrict__ wv,
                                               const char* __restrict__ woA, const char* __restrict__ woB,
                                               char* __restrict__ ws) {
  extern __shared__ __attribute__((aligned(16))) char smem[];
  asm volatile("" ::"s"(xA), "s"(wqkv), "s"(woA), "s"(xB), "s"(wq), "s"(wk), "s"(wv), "s"(woB), "s"(ws));
  const int tid = threadIdx.x, lane = tid & 63;
  const int wave = __builtin_amdgcn_readfirstlane(tid >> 6);

  const unsigned lds0 = (unsigned)(uintptr_t)smem;
  const int b = blockIdx.x;
  const int unit = (b & 7) * 32 + (b >> 3);
  const int fr = lane & 15, kg = lane >> 4;
  const int rot = unit % 9;
  const int tro = kg * 2048 + (fr >> 2) * 64 + (fr & 3) * 8;
  const int trE = K1_XB + tro + ((kg & 1) << 5), trO = K1_XB + tro + (((kg & 1) ^ 1) << 5);

  if (__builtin_expect(unit >= K1_GEMM, 0)) {
    const int s = unit - K1_GEMM;
    if (s == 0 && tid < 256) *(unsigned*)(ws + WS_CNT + tid * 128) = 0u;
    if (wave >= 4) {
      const int c0 = s * 128, c1 = c0 + 64;
      k1_loader<2>(nullptr, 0, c0 < DM ? woA : woB, c1 < DM ? woA : woB, ROWB, (c0 < DM ? c0 : c0 - DM) * 2, (c1 < DM ? c1 : c1 - DM) * 2,
                   nullptr, nullptr, rot, lds0, wave - 4, lane);
    } else {
      bar_lgkm();
      char* dst = ws + WS_WF + (size_t)(s * 8 + wave * 2) * 18432 + lane * 16;
#pragma unroll 1
      for (int j = 0; j < 9; ++j) {
        int ks = j + rot; ks = ks >= 9 ? ks - 9 : ks;
        const char* sb = smem + (j & 3) * K1_STAGE + wave * 512;
        const bf16x8 f0 = tr_frag(sb + trE), f1 = tr_frag(sb + trO), f2 = tr_frag(sb + trE + 8192), f3 = tr_frag(sb + trO + 8192);
        bar_lgkm();
        ST16K(dst + ks * 2048, __builtin_bit_cast(u32x4, f0));
        ST16K(dst + ks * 2048 + 18432, __builtin_bit_cast(u32x4, f1));
        ST16K(dst + ks * 2048 + 1024, __builtin_bit_cast(u32x4, f2));
        ST16K(dst + ks * 2048 + 18432 + 1024, __builtin_bit_cast(u32x4, f3));
      }
    }

    return;
  }
  const bool isA = unit < K1_RB * K1_ACT;
  const int u5 = isA ? unit : unit - K1_RB * K1_ACT, d5 = (u5 * 205) >> 10, m5 = u5 - d5 * 5;
  const int v4 = unit - (unit >= 126 ? 126 : 70);
  const bool five = !isA || unit < 70;
  const int ct = five ? (isA ? d5 : m5) : v4 >> 2;
  const int rb = five ? (isA ? m5 : d5) : (unit >= 126 ? 9 : 5) + (v4 & 3);
  const int row0 = rb * 160;
  const bool last = ct == (isA ? K1_ACT - 1 : K1_BCT - 1);
  const int wm = wave & 1, wn = (wave >> 1) & 1;
  constexpr int ST_LD = 144;
  if (__builtin_expect(wave >= 4, 1)) {
    if (__builtin_expect(isA || !last, 1))
      k1_loader<0>(isA ? xA : xB, row0, isA ? wqkv : wq, isA ? wqkv : wq, isA ? 3 * ROWB : ROWB, ct * 256, ct * 256 + (last ? 0 : 128),
                   nullptr, nullptr, rot, lds0, wave - 4, lane);
    else k1_loader<1>(xB, row0, wq, wq, ROWB, ct * 256, 0, wk, wv, rot, lds0, wave - 4, lane);
  } else {
    f32x4 acc[5][4] = {};
    const int xo0 = (wm * 80 + fr) * 128 + ((kg ^ (fr & 7)) << 4), xo1 = xo0 ^ 64;
    const int wE = trE + wn * 1024, wO = trO + wn * 1024;
    bf16x8 xa[5], wa[4], xb[5], wb[4];
    auto rd = [&](const char* sb, int kk, bf16x8 (&xf)[5], bf16x8 (&wf)[4]) {
#pragma unroll
      for (int m = 0; m < 5; ++m) xf[m] = *(const bf16x8*)(sb + (kk ? xo1 : xo0) + m * 2048);
#pragma unroll
      for (int n = 0; n < 4; ++n) wf[n] = tr_frag(sb + ((n & 1) ? wO : wE) + kk * 8192 + (n >> 1) * 512);
    };
    auto mm = [&](bf16x8 (&xf)[5], bf16x8 (&wf)[4]) {
#pragma unroll
      for (int n = 0; n < 4; ++n)
#pragma unroll
        for (int m = 0; m < 5; ++m) acc[m][n] = MFMA16(wf[n], xf[m], acc[m][n]);
    };
    bar_lgkm();

    rd(smem, 0, xa, wa);
#define ILV() do { _Pragma("unroll") for (int q_ = 0; q_ < 13; ++q_) { __builtin_amdgcn_sched_group_barrier(0x8, 1, 0); __builtin_amdgcn_sched_group_barrier(0x100, 1, 0); } \
                   __builtin_amdgcn_sched_group_barrier(0x8, 7, 0); } while (0)
#pragma unroll
    for (int j = 0; j < 9; ++j) {
      rd(smem + (j & 3) * K1_STAGE, 1, xb, wb);
      mm(xa, wa);
      ILV();
      __builtin_amdgcn_sched_barrier(0);
      bar_lgkm();
      rd(smem + ((j + 1) & 3) * K1_STAGE, 0, xa, wa);
      mm(xb, wb);
      ILV();
      __builtin_amdgcn_sched_barrier(0);
    }

    char* st = smem + wave * (80 * ST_LD);
#pragma unroll
    for (int m = 0; m < 5; ++m)
#pragma unroll
      for (int n = 0; n < 4; ++n) {
        u32x2 u = {pk2(acc[m][n][0], acc[m][n][1]), pk2(acc[m][n][2], acc[m][n][3])};
        *(u32x2*)(st + (m * 16 + fr) * ST_LD + n * 32 + kg * 8) = u;
      }
    asm volatile("s_waitcnt lgkmcnt(0)" ::: "memory");

    if (__builtin_expect(!(last && wn == 1), 1)) {
      const int ld = isA ? 3 * ROWB : ROWB, r0 = row0 + wm * 80 + (lane >> 3);
      char* d = (isA ? ws + WS_QKV : ws + WS_QB) + (size_t)r0 * ld + ct * 256 + wn * 128 + (lane & 7) * 16;
      u32x4 v[10];
#pragma unroll
      for (int i = 0; i < 10; ++i) v[i] = *(const u32x4*)(st + (i * 8 + (lane >> 3)) * ST_LD + (lane & 7) * 16);
#pragma unroll
      for (int i = 0; i < 10; ++i) if (r0 + i * 8 < SEQ) ST16K(d + (size_t)i * 8 * ld, v[i]);
    }
  }
  if (__builtin_expect(!isA && last, 0)) {
    bar_lgkm();
    const char* st = smem + 2 * (80 * ST_LD);
    char* kv = ws + WS_KV;
    if (tid < 320) {
      const int r = tid >> 1, hh = tid & 1, key = row0 + r;
      if (key < SEQ) {
        const int t = key >> 5, k32 = key & 31;
        char* g = kv + (t >> 2) * GRP_B;
        ST16K(g + (t & 3) * 1024 + (hh * 32 + k32) * 16, *(const u32x4*)(st + r * ST_LD + hh * 16));
        if (hh == 0) *(unsigned*)(g + KR_OFF + k32 * 16 + (t & 3) * 4) = *(const unsigned*)(st + r * ST_LD + 32);
      }
    }
    const int t2 = 511 - tid;
    if (t2 < 400) {
      const int blk = t2 / 20, d = t2 - blk * 20;
      const int tl = blk >> 2, s = (blk >> 1) & 1, hh = blk & 1;
      const int t = (row0 >> 5) + tl;
      if (t < 64) {
        const char* src = st + (tl * 32 + s * 16 + hh * 4) * ST_LD + 64 + (d < 18 ? d : 0) * 2;
        u32x4 o;
#pragma unroll
        for (int e = 0; e < 4; ++e) {
          const int r = (e >> 1) * 8 + (e & 1) * 2;
          o[e] = (unsigned)*(const unsigned short*)(src + r * ST_LD) | ((unsigned)*(const unsigned short*)(src + (r + 1) * ST_LD) << 16);
          o[e] = d < 18 ? o[e] : (d == 18 ? 0x3f803f80u : 0u);
        }
        ST16K(kv + (t >> 2) * GRP_B + V_OFF + (t & 3) * VT_B + (s * 2 + hh) * 320 + d * 16, o);
      }
    }
  }

}

constexpr int K2_LDS = KV_BYTES + 6144;
constexpr float M_OFF = 96.0f;
#define SB __builtin_amdgcn_sched_barrier(0)
#define MF16(a, b, c) __builtin_amdgcn_mfma_f32_32x32x16_bf16(a, b, c, 0, 0, 0)
#define MF8(a, b, c) __builtin_amdgcn_mfma_f32_32x32x8bf16_1k(a, b, c, 0, 0, 0)
#define EX(i) cur[i] = __builtin_amdgcn_exp2f(cur[i])

DI float xhalf(float x);
DI float max16(const f32x16& s, float m) {
#pragma unroll
  for (int i = 0; i < 16; i += 2) m = __builtin_fmaxf(__builtin_fmaxf(s[i], s[i + 1]), m);
  return m;
}
#define E(i) EX(i); SB;
#define C0(i) p0[i] = pk2(cur[2 * i], cur[2 * i + 1]); SB;
#define C1(i) p1[i] = pk2(cur[8 + 2 * i], cur[9 + 2 * i]); SB;
template <bool RELOAD>
DI void phase(f32x16& cur, f32x16& nxt, s16x4& kra, const char* krp, const s16x4 qr, bf16x8& kf, const char* kp2, const bf16x8 qf,
              bf16x8& v0, const char* v0p, bf16x8& v1, const char* v1p, u32x4& p0, u32x4& p1, f32x16& o) {
  const f32x16 z = {};
  nxt = MF8(kra, qr, z); SB;
  if (RELOAD) { kra = *(const s16x4*)krp; SB; }
  E(0) E(1) E(2) E(3) C0(0) E(4)
  o = MF16(v1, __builtin_bit_cast(bf16x8, p1), o); SB;
  v1 = *(const bf16x8*)v1p; SB;
  C0(1) E(5) E(6) E(7) C0(2) E(8)
  nxt = MF16(kf, qf, nxt); SB;
  kf = *(const bf16x8*)kp2; SB;
  C0(3) E(9) E(10) E(11) C1(0) E(12)
  o = MF16(v0, __builtin_bit_cast(bf16x8, p0), o); SB;
  v0 = *(const bf16x8*)v0p; SB;
  C1(1) E(13) E(14) E(15) C1(2) C1(3)
}
DI unsigned rem_word(unsigned qrem, float m, int h) {
  const unsigned hi = pk2(-m, 0.f) & 0xffffu;
  const unsigned lo = pk2(-m - bf_lo(hi), 0.f) & 0xffffu;
  return h ? (hi | (lo << 16)) : qrem;
}
DI s16x4 mk2(unsigned a, unsigned b) { u32x2 r = {a, b}; return __builtin_bit_cast(s16x4, r); }

DI float max_pass(const char* kp, const char* krp, const bf16x8 qf, unsigned rw, int npair) {
  const s16x4 qe = mk2(rw, 0u), qo = mk2(0u, rw);
  const f32x16 z = {};
  float m = -3.0e38f;
  for (int i = 0; i < npair; ++i) {
    const int G = (i >> 1) * GRP_B, hf = i & 1;
    const s16x4 kr = *(const s16x4*)(krp + G + hf * 8);
    m = max16(MF16(*(const bf16x8*)(kp + G + hf * 2048), qf, MF8(kr, qe, z)), m);
    m = max16(MF16(*(const bf16x8*)(kp + G + hf * 2048 + 1024), qf, MF8(kr, qo, z)), m);
  }
  return fmaxf(m, xhalf(m));
}
DI void attn_exact(const char* img, int ko, int kro, int vo, const bf16x8 qf, unsigned qrem, int h, f32x16& o) {
  const float mt = max_pass(img + ko, img + kro, qf, rem_word(qrem, 0.f, h), 32);
  const unsigned rw = rem_word(qrem, mt, h);
  const s16x4 qe = mk2(rw, 0u), qo = mk2(0u, rw);
  const f32x16 z = {};
  o = z;
  for (int t = 0; t < 64; ++t) {
    const char* g = img + (t >> 2) * GRP_B;
    const s16x4 kr = *(const s16x4*)(g + kro + (t & 2) * 4);
    f32x16 cur = MF16(*(const bf16x8*)(g + ko + (t & 3) * 1024), qf, MF8(kr, (t & 1) ? qo : qe, z));
    u32x4 p0, p1;
#pragma unroll
    for (int i = 0; i < 16; ++i) EX(i);
#pragma unroll
    for (int i = 0; i < 4; ++i) { p0[i] = pk2(cur[2 * i], cur[2 * i + 1]); p1[i] = pk2(cur[8 + 2 * i], cur[9 + 2 * i]); }
    o = MF16(*(const bf16x8*)(g + vo + (t & 3) * VT_B), __builtin_bit_cast(bf16x8, p0), o);
    o = MF16(*(const bf16x8*)(g + vo + (t & 3) * VT_B + 640), __builtin_bit_cast(bf16x8, p1), o);
  }
}

constexpr int LA_0 = 98304;
constexpr int LA_Q = LA_0 + 22 * 2304;
constexpr int L_OB = 94208;
constexpr int L_P = 104448;
constexpr int P_LD = 272, P_HALF = 80 * P_LD;
constexpr int XC_B = 10240;
constexpr int L_CTL = K2_LDS;
constexpr int K2_LDS_ALL = K2_LDS + 64;
enum { C_KVA = 0, C_KVB = 4, C_PAST = 8, C_AROWS = 12, C_A2 = 16, C_XA = 20, C_DONE = 24 };

DI void lds_add(unsigned addr) {
  asm volatile("s_waitcnt lgkmcnt(0)\n\ts_mov_b64 exec, 1\n\tds_add_u32 %0, %1\n\ts_mov_b64 exec, -1" ::"v"(addr), "v"(1u) : "memory");
}
template <int N> DI void lds_wait(unsigned addr) {
  unsigned r, t;
  asm volatile("1:\n\tds_read_b32 %0, %2\n\ts_waitcnt lgkmcnt(0)\n\tv_readfirstlane_b32 %1, %0\n\ts_cmp_lt_u32 %1, %3\n\ts_cbranch_scc1 1b"
               : "=&v"(r), "=&s"(t) : "v"(addr), "i"(N) : "memory", "scc");
}
DI void cnt_wait(const char* p) {
  unsigned r, n;
  asm volatile("s_mov_b32 %1, 0x400000\n1:\n\tglobal_load_dword %0, %2, off sc1\n\ts_sub_u32 %1, %1, 1\n\ts_cbranch_scc1 2f\n\t"
               "s_waitcnt vmcnt(0)\n\tv_cmp_gt_u32 vcc, 16, %0\n\ts_cbranch_vccz 2f\n\ts_sleep " POLL_SLEEP "\n\ts_branch 1b\n2:\n\ts_waitcnt vmcnt(0)"
               : "=&v"(r), "=&s"(n) : "v"(p) : "memory", "scc", "vcc");
}
DI float xhalf(float x) {
  const unsigned u = __float_as_uint(x);
  const auto r = __builtin_amdgcn_permlane32_swap(u, u, false, false);
  return __uint_as_float((threadIdx.x & 32) ? r[0] : r[1]);
}
DI void local_attn(char* smem, unsigned lds0, int wave, int c32, int h, int row) {
  unsigned* qp = (unsigned*)(smem + LA_Q + wave * ROWB + c32 * 36);
  unsigned q[9];
#pragma unroll
  for (int e = 0; e < 9; ++e) q[e] = qp[e];
  const unsigned kb = lds0 + LA_0 + (wave + 8 - h * 8) * 2304 + c32 * 36;
  const unsigned vb = lds0 + LA_0 + wave * 2304 + ROWB + c32 * 36 + h * 16;
  u32x2 kk[9][2], vv[5][4];
#pragma unroll
  for (int e = 0; e < 9; ++e)
    asm volatile("ds_read2st64_b32 %0, %2 offset0:54 offset1:36\n\tds_read2st64_b32 %1, %2 offset0:18 offset1:0"
                 : "=&v"(kk[e][0]), "=&v"(kk[e][1]) : "v"(kb + e * 4) : "memory");
  asm volatile("s_waitcnt lgkmcnt(0)" : "+v"(kk[0][0]), "+v"(kk[0][1]), "+v"(kk[1][0]), "+v"(kk[1][1]), "+v"(kk[2][0]), "+v"(kk[2][1]),
      "+v"(kk[3][0]), "+v"(kk[3][1]), "+v"(kk[4][0]), "+v"(kk[4][1]), "+v"(kk[5][0]), "+v"(kk[5][1]), "+v"(kk[6][0]), "+v"(kk[6][1]),
      "+v"(kk[7][0]), "+v"(kk[7][1]), "+v"(kk[8][0]), "+v"(kk[8][1])::"memory");
#pragma unroll
  for (int e = 0; e < 5; ++e)
    asm volatile("ds_read2st64_b32 %0, %4 offset0:126 offset1:108\n\tds_read2st64_b32 %1, %4 offset0:90 offset1:72\n\t"
                 "ds_read2st64_b32 %2, %4 offset0:54 offset1:36\n\tds_read2st64_b32 %3, %4 offset0:18 offset1:0"
                 : "=&v"(vv[e][0]), "=&v"(vv[e][1]), "=&v"(vv[e][2]), "=&v"(vv[e][3]) : "v"(vb + e * 4) : "memory");
  float lg[4] = {0.f, 0.f, 0.f, 0.f};
#pragma unroll
  for (int e = 0; e < 9; ++e) {
    const unsigned k0 = kk[e][0][0], k1 = kk[e][0][1], k2 = kk[e][1][0], k3 = kk[e][1][1];
    lg[0] = dot2(q[e], k0, lg[0]); lg[1] = dot2(q[e], k1, lg[1]); lg[2] = dot2(q[e], k2, lg[2]); lg[3] = dot2(q[e], k3, lg[3]);
  }
#pragma unroll
  for (int w = 0; w < 4; ++w) lg[w] = row - 2 * (h * 4 + w) >= 0 ? lg[w] * QSCALE : -1.0e9f * LOG2E;
  float mx = fmaxf(fmaxf(lg[0], lg[1]), fmaxf(lg[2], lg[3]));
  mx = fmaxf(mx, xhalf(mx));
  float sum = 0.f;
#pragma unroll
  for (int w = 0; w < 4; ++w) { lg[w] = __builtin_amdgcn_exp2f(lg[w] - mx); sum += lg[w]; }
  sum += xhalf(sum);
  const float inv = __builtin_amdgcn_rcpf(sum);
  const unsigned pa = pk2(lg[0] * inv, lg[1] * inv), pb = pk2(lg[2] * inv, lg[3] * inv);
  const auto ra = __builtin_amdgcn_permlane32_swap(pa, pa, false, false);
  const auto rb = __builtin_amdgcn_permlane32_swap(pb, pb, false, false);
  const unsigned pp[4] = {ra[0], rb[0], ra[1], rb[1]};
  asm volatile("s_waitcnt lgkmcnt(0)" : "+v"(vv[0][0]), "+v"(vv[0][1]), "+v"(vv[0][2]), "+v"(vv[0][3]), "+v"(vv[1][0]), "+v"(vv[1][1]),
      "+v"(vv[1][2]), "+v"(vv[1][3]), "+v"(vv[2][0]), "+v"(vv[2][1]), "+v"(vv[2][2]), "+v"(vv[2][3]), "+v"(vv[3][0]), "+v"(vv[3][1]),
      "+v"(vv[3][2]), "+v"(vv[3][3]), "+v"(vv[4][0]), "+v"(vv[4][1]), "+v"(vv[4][2]), "+v"(vv[4][3])::"memory");
#pragma unroll
  for (int e = 0; e < 5; ++e) {
    float o0 = 0.f, o1 = 0.f;
#pragma unroll
    for (int w2 = 0; w2 < 4; ++w2) {
      const unsigned x = vv[e][w2][0], y = vv[e][w2][1];
      o0 = dot2(__builtin_amdgcn_perm(y, x, 0x05040100u), pp[w2], o0);
      o1 = dot2(__builtin_amdgcn_perm(y, x, 0x07060302u), pp[w2], o1);
    }
    qp[h * 4 + e] = pk2(o0, o1);
  }
}
template <int MASK> DI void glds16_part(unsigned voff, const void* sbase, unsigned lds_dst) {
  unsigned keep;
  asm volatile("s_mov_b32 %0, m0\n\ts_mov_b32 m0, %3\n\ts_mov_b64 exec, %4\n\tglobal_load_lds_dwordx4 %1, %2\n\ts_mov_b64 exec, -1\n\ts_mov_b32 m0, %0"
               : "=&s"(keep) : "v"(voff), "s"(sbase), "s"(lds_dst), "i"(MASK) : "memory");
}
DI void glds16_sc1(unsigned voff, const void* sbase, unsigned lds_dst) {
  unsigned keep;
  asm volatile("s_mov_b32 %0, m0\n\ts_mov_b32 m0, %3\n\ts_nop 0\n\tglobal_load_lds_dwordx4 %1, %2 sc1\n\ts_mov_b32 m0, %0"
               : "=&s"(keep) : "v"(voff), "s"(sbase), "s"(lds_dst) : "memory");
}
DI void store_row(const char* stage, char* dst, int lane) {
  asm volatile("s_waitcnt lgkmcnt(0)" ::: "memory");
  const u32x4 a = *(const u32x4*)(stage + lane * 16);
  const u32x4 b = *(const u32x4*)(stage + 1024 + (lane & 7) * 16);
  asm volatile("global_store_dwordx4 %0, %1, off sc1\n\ts_mov_b64 exec, 0xff\n\tglobal_store_dwordx4 %0, %2, off offset:1024 sc1\n\t"
               "s_mov_b64 exec, -1\n\ts_nop 0" ::"v"(dst + lane * 16), "v"(a), "v"(b) : "memory");
}
DI void signal_row(char* cnt) {
  asm volatile("s_waitcnt vmcnt(0)\n\ts_mov_b64 exec, 1\n\tglobal_atomic_add %0, %1, off sc1\n\ts_mov_b64 exec, -1" ::"v"(cnt), "v"(1u) : "memory");
}

__global__ __launch_bounds__(512) void k2_attn(char* __restrict__ ws, char* __restrict__ out) {
  extern __shared__ __attribute__((aligned(16))) char smem[];
  asm volatile("" ::"s"(ws), "s"(out));
  const int tid = threadIdx.x, lane = tid & 63;
  const int wave = __builtin_amdgcn_readfirstlane(tid >> 6);

  const int b = blockIdx.x;
  const int rblk = (b & 7) * 32 + (b >> 3);
  const int row = rblk * 8 + wave;
  const int c32 = lane & 31, h = lane >> 5;
  const unsigned lds0 = (unsigned)(uintptr_t)smem;
  const unsigned ctl = lds0 + L_CTL;
  const int ko = lane * 16;
  auto kv_fetch = [&](int p0, int step, int n) {
#pragma unroll
    for (int i = 0; i < n; ++i) glds16(ko, ws + WS_KV + (p0 + step * i) * 1024, lds0 + (p0 + step * i) * 1024);
  };
  u32x4 qv;
  unsigned qrem;
  {
    const char* qp = ws + WS_QB + (size_t)row * ROWB + c32 * 36;
    asm volatile("global_load_dwordx4 %0, %1, off" : "=v"(qv) : "v"(qp + h * 16) : "memory");
    asm volatile("global_load_dword %0, %1, off offset:32" : "=v"(qrem) : "v"(qp) : "memory");
    kv_fetch(wave, 8, 5);
  }
  if (tid < 16) *(unsigned*)(smem + L_CTL + tid * 4) = 0u;
  const int vrow = c32 < 19 ? c32 : (c32 == 28 ? 18 : 19);
  const int kro = h ? V_OFF + 18 * 16 : KR_OFF + c32 * 16;
  const int vo = V_OFF + h * 320 + vrow * 16;
  const int rg = rblk / 9, cg = rblk - rg * 9;
  const int nrt = rg < 16 ? 5 : (rg < 28 ? 4 : 0), rt0 = rg < 16 ? rg * 5 : (rg < 28 ? 16 + rg * 4 : 0);
  const int fr = lane & 15, kg = lane >> 4;
  const unsigned xo = (lane >> 3) * ROWB + (((lane & 7) ^ (lane >> 3)) << 4);
  auto fetch_tile = [&](const char* src) {
    const char* s0 = src + (size_t)(rt0 * 16 + wave * 8) * ROWB;
    const char* s1 = src + (size_t)(rt0 * 16 + (nrt == 5 ? 64 : 0) + (wave & 1) * 8) * ROWB;
#pragma unroll
    for (int c = 0; c < 9; ++c) {
      glds16_sc1(xo, s0 + c * 128, lds0 + c * XC_B + wave * 1024);
      if (c % 3 == 2) {
        const int ce = c == 8 ? 8 : (c / 3) * 4 + (wave >> 1);
        glds16_sc1(xo, s1 + ce * 128, lds0 + ce * XC_B + (8 + (wave & 1)) * 1024);
      }
    }
  };
  const char* cnt_p = ws + WS_CNT + (rt0 + (lane < (nrt == 5 ? 5 : 4) ? lane : 0)) * 128;
  const char* wfa = ws + WS_WF + (size_t)(cg * 4 + (wave & 3)) * 18432 + (wave >> 2) * 1024;
  const f32x16 z = {};
  f32x16 o = z;
  u32x4 wA[9], wB[9];

  asm volatile("s_waitcnt vmcnt(5)" : "+v"(qv), "+v"(qrem)::"memory");
#pragma unroll
  for (int e = 0; e < 4; ++e) { const unsigned u = qv[e]; qv[e] = pk2(bf_lo(u) * QSCALE, bf_hi(u) * QSCALE); }
  qrem = pk2(bf_lo(qrem) * QSCALE, bf_hi(qrem) * QSCALE);
  const bf16x8 qf = __builtin_bit_cast(bf16x8, qv);
  wait_vm<0>();
  bar_lgkm();

  if (wave >= 4) {
    const int w4 = wave - 4;
    kv_fetch(40 + w4, 4, 14);
#pragma unroll
    for (int i = 0; i < 6; ++i) {
      int r = w4 + 4 * i;
      r = r > 21 ? 21 : r;
      const int gr = rblk * 8 + r - 14;
      const char* src = ws + WS_QKV + (size_t)(gr < 0 ? 0 : gr) * (3 * ROWB) + ROWB;
      glds16(ko, src, lds0 + LA_0 + r * 2304);
      glds16(ko, src + 1024, lds0 + LA_0 + r * 2304 + 1024);
      glds16_part<0xffff>(ko, src + 2048, lds0 + LA_0 + r * 2304 + 2048);
    }
#pragma unroll
    for (int i = 0; i < 2; ++i) {
      const char* src = ws + WS_QKV + (size_t)(rblk * 8 + w4 + 4 * i) * (3 * ROWB);
      glds16(ko, src, lds0 + LA_Q + (w4 + 4 * i) * ROWB);
      glds16_part<0xff>(ko, src + 1024, lds0 + LA_Q + (w4 + 4 * i) * ROWB + 1024);
    }
    wait_vm<22>();
    lds_add(ctl + C_KVA);
  }
  const float ms = max_pass(smem + ko, smem + kro, qf, rem_word(qrem, 0.f, h), 1);
  const unsigned rw = rem_word(qrem, ms + M_OFF, h);
  {
    int kq = ko, krq = kro, vq = vo;
    const char *kp = smem + kq, *krp = smem + krq, *vp = smem + vq;
    const s16x4 qe = mk2(rw, 0u), qo = mk2(0u, rw);
    s16x4 k01 = *(const s16x4*)krp, k23 = *(const s16x4*)(krp + 8);
    f32x16 sa = MF16(*(const bf16x8*)kp, qf, MF8(k01, qe, z)), sb;
    bf16x8 kf = *(const bf16x8*)(kp + 1024), v0 = *(const bf16x8*)vp, v1 = v0;
    u32x4 p0 = {}, p1 = {};
    unsigned cnt_seen;

#define WF9(W, BASE) asm volatile( \
        "global_load_dwordx4 %0, %9, %10\n\tglobal_load_dwordx4 %1, %9, %10 offset:2048\n\t" \
        "global_load_dwordx4 %2, %11, %10 offset:-4096\n\tglobal_load_dwordx4 %3, %11, %10 offset:-2048\n\t" \
        "global_load_dwordx4 %4, %11, %10\n\tglobal_load_dwordx4 %5, %11, %10 offset:2048\n\t" \
        "global_load_dwordx4 %6, %12, %10 offset:-4096\n\tglobal_load_dwordx4 %7, %12, %10 offset:-2048\n\t" \
        "global_load_dwordx4 %8, %12, %10" \
        : "=&v"(W[0]), "=&v"(W[1]), "=&v"(W[2]), "=&v"(W[3]), "=&v"(W[4]), "=&v"(W[5]), "=&v"(W[6]), "=&v"(W[7]), "=&v"(W[8]) \
        : "v"(ko), "s"(BASE), "v"(ko + 8192), "v"(ko + 16384) : "memory")
    auto group = [&](auto gc) __attribute__((always_inline)) {
      constexpr int g = decltype(gc)::value;

      if constexpr (PRIO(g) != PRIO(g - 1)) __builtin_amdgcn_s_setprio(PRIO(g));
      if constexpr (g == 1) { wait_vm<0>(); lds_add(ctl + C_AROWS); }
      if constexpr (g == 3) lds_wait<4>(ctl + C_KVA);
      if constexpr (g == 3 || g == 4) {
        if ((wave >> 2) == 4 - g) {
          lds_wait<8>(ctl + C_AROWS);
          local_attn(smem, lds0, wave, c32, h, row);
          store_row(smem + LA_Q + wave * ROWB, ws + WS_OA + (size_t)row * ROWB, lane);
          lds_add(ctl + C_A2);
        }
      }
      if constexpr (g == 6) {
        signal_row(ws + WS_CNT + (row >> 4) * 128);
        lds_wait<8>(ctl + C_A2);
        kv_fetch(96 + wave, 8, 7);
      }
      if constexpr (g == 7) { wait_vm<0>(); lds_add(ctl + C_KVB); }
      if constexpr (g == 8) WF9(wA, wfa);
      if constexpr (g == 9) lds_wait<8>(ctl + C_KVB);
      if constexpr (g == 10) WF9(wB, wfa + 36 * 18432);
      if constexpr (g == 11) lds_add(ctl + C_PAST);
      if constexpr (g == 12) asm volatile("global_load_dword %0, %1, off sc1" : "=v"(cnt_seen) : "v"(cnt_p) : "memory");
      if constexpr (g == 13) {
        lds_wait<8>(ctl + C_PAST);
        asm volatile("s_waitcnt vmcnt(0)" : "+v"(cnt_seen)::"memory");
        if (__builtin_expect(!__all(cnt_seen >= 16u), 0)) cnt_wait(cnt_p);
        fetch_tile(ws + WS_OA);
      }
      if constexpr (g == 15) { wait_vm<0>(); lds_add(ctl + C_XA); }

      SB;
      const int G = (g & 3) * GRP_B;
      phase<true>(sa, sb, k01, krp + G + GRP_B, qo, kf, kp + G + 2048, qf, v0, vp + G + VT_B, v1, vp + G + 640, p0, p1, o);
      phase<false>(sb, sa, k23, nullptr, qe, kf, kp + G + 3072, qf, v0, vp + G + 2 * VT_B, v1, vp + G + VT_B + 640, p0, p1, o);
      phase<true>(sa, sb, k23, krp + G + GRP_B + 8, qo, kf, kp + G + GRP_B, qf, v0, vp + G + 3 * VT_B, v1, vp + G + 2 * VT_B + 640, p0, p1, o);
      phase<false>(sb, sa, k01, nullptr, qe, kf, kp + G + GRP_B + 1024, qf, v0, vp + G + GRP_B, v1, vp + G + 3 * VT_B + 640, p0, p1, o);
      asm volatile("" : "+v"(o), "+v"(sa), "+v"(p1));
      if constexpr ((g & 3) == 3) {
        kq += 4 * GRP_B; krq += 4 * GRP_B; vq += 4 * GRP_B;
        asm volatile("" : "+v"(kq), "+v"(krq), "+v"(vq));
        kp = smem + kq; krp = smem + krq; vp = smem + vq;
      }
    };
#define GR(i) group(IC<i>{});
    GR(0) GR(1) GR(2) GR(3) GR(4) GR(5) GR(6) GR(7) GR(8) GR(9) GR(10) GR(11) GR(12) GR(13) GR(14) GR(15)
    o = MF16(v1, __builtin_bit_cast(bf16x8, p1), o);
  }

  lds_add(ctl + C_DONE);
  float l = h ? o[12] : o[10];
  float big = l;
#pragma unroll
  for (int i = 0; i < 10; ++i) big += fabsf(o[i]);
  const bool ok = big < 1.0e37f && l > 1.0e-33f;
  if (__builtin_expect(!__all(ok), 0)) {
    attn_exact(ws + WS_KV, ko, kro, vo, qf, qrem, h, o);
    l = h ? o[12] : o[10];
  }
  {
    const float inv = __builtin_amdgcn_rcpf(l);
    unsigned* dst = (unsigned*)(smem + L_OB + wave * ROWB + c32 * 36 + h * 8);
    dst[0] = pk2(o[0] * inv, o[1] * inv);
    dst[1] = pk2(o[2] * inv, o[3] * inv);
    dst[4] = pk2(o[4] * inv, o[5] * inv);
    dst[5] = pk2(o[6] * inv, o[7] * inv);
    if (!h) dst[8] = pk2(o[8] * inv, o[9] * inv);
    store_row(smem + L_OB + wave * ROWB, ws + WS_OB + (size_t)row * ROWB, lane);
  }
  char* cntB = ws + WS_CNT + (128 + (row >> 4)) * 128;
  const int xa = fr * 128 + ((((wave >> 2) * 4 + kg) ^ (fr & 7)) << 4);
  char* pw = smem + L_P + (wave >> 2) * P_HALF + fr * P_LD + (wave & 3) * 64 + kg * 16;
  f32x4 acc[5];
  auto mm3 = [&](u32x4 (&w)[9], int c0) {
    bf16x8 xf[3][5];
#pragma unroll
    for (int c = 0; c < 3; ++c)
#pragma unroll
      for (int m = 0; m < 5; ++m) xf[c][m] = *(const bf16x8*)(smem + (c0 + c) * XC_B + xa + m * 2048);
    SB;
#pragma unroll
    for (int c = 0; c < 3; ++c)
#pragma unroll
      for (int m = 0; m < 5; ++m) acc[m] = MFMA16(__builtin_bit_cast(bf16x8, w[c0 + c]), xf[c][m], (c0 + c) ? acc[m] : f32x4{});
  };
  auto put = [&] {
#pragma unroll
    for (int m = 0; m < 5; ++m) *(f32x4*)(pw + m * 16 * P_LD) = acc[m];
  };
  auto tile_out = [&](int br) {
#pragma unroll
    for (int i = 0; i < 2; ++i) {
      const int r = (tid >> 3) + 64 * i, c = tid & 7;
      if (r < nrt * 16) {
        const char* p = smem + L_P + r * P_LD + c * 32;
        const f32x4 a0 = *(const f32x4*)p, a1 = *(const f32x4*)(p + 16);
        const f32x4 b0 = *(const f32x4*)(p + P_HALF), b1 = *(const f32x4*)(p + P_HALF + 16);
        const u32x4 u = {pk2(a0[0] + b0[0], a0[1] + b0[1]), pk2(a0[2] + b0[2], a0[3] + b0[3]),
                         pk2(a1[0] + b1[0], a1[1] + b1[1]), pk2(a1[2] + b1[2], a1[3] + b1[3])};
        ST16K(out + (size_t)(rt0 * 16 + r) * (2 * ROWB) + br * ROWB + cg * 128 + c * 16, u);
      }
    }
  };
  signal_row(cntB);

  lds_wait<8>(ctl + C_XA);
  mm3(wA, 0);
  mm3(wA, 3);
  mm3(wA, 6);

  lds_wait<8>(ctl + C_DONE);
  put();

  if (wave == 0) cnt_wait(cnt_p + 128 * 128);
  bar_lgkm();

  fetch_tile(ws + WS_OB);

  tile_out(0);

  wait_vm<8>();
  bar_lgkm();

  mm3(wB, 0);
  wait_vm<4>();
  bar_lgkm();
  mm3(wB, 3);
  wait_vm<0>();
  bar_lgkm();
  mm3(wB, 6);
  put();
  bar_lgkm();

  tile_out(1);

}

extern "C" void kernel_launch(void* const* d_in, const int* in_sizes, int n_in, void* d_out, int out_size,
                              void* d_ws, size_t ws_size, hipStream_t stream) {
  (void)in_sizes; (void)n_in; (void)out_size; (void)ws_size;
  (void)hipFuncSetAttribute((const void*)k1_proj, hipFuncAttributeMaxDynamicSharedMemorySize, K1_LDS);
  (void)hipFuncSetAttribute((const void*)k2_attn, hipFuncAttributeMaxDynamicSharedMemorySize, K2_LDS_ALL);
  char* ws = (char*)d_ws;
  k1_proj<<<256, 512, K1_LDS, stream>>>((const char*)d_in[0], (const char*)d_in[1], (const char*)d_in[3], (const char*)d_in[4],
                                        (const char*)d_in[5], (const char*)d_in[6], (const char*)d_in[2], (const char*)d_in[7], ws);
  k2_attn<<<256, 512, K2_LDS_ALL, stream>>>(ws, (char*)d_out);
}
```
